```python
import torch

def _softmax(x, axis=-1):
    return torch.softmax(x, dim=axis)
def _gelu(x):
    return torch.nn.functional.gelu(x, approximate="tanh")
def _silu(x):
    return torch.nn.functional.silu(x)
def _sigmoid(x):
    return torch.sigmoid(x)
def _layernorm(x, axis=-1, eps=1e-5):
    m = x.mean(dim=axis, keepdim=True)
    v = x.var(dim=axis, keepdim=True, unbiased=False)
    return (x - m) / torch.sqrt(v + eps)
def _scatter_add(tgt, idx, src, axis=0):
    return tgt.clone().index_add_(axis, idx, src)
def _segment_sum(src, seg, n):
    out = torch.zeros((n,) + src.shape[1:], dtype=src.dtype)
    return out.index_add_(0, seg, src)
def _pad(x, pw):
    flat = tuple(v for pair in reversed(pw) for v in pair)
    return torch.nn.functional.pad(x, flat)

kernel_name = 'parallel_sum__mha_block_cached__glu_gated_glu__decode'

def setup_inputs():

    _w = (lambda a: (lambda t: t.to(torch.bfloat16) if t.is_floating_point() else t)(torch.as_tensor(a)))
    def _A_setup():
        B, Sq, D = 1, 1, 512
        H, Sk, Dh = 32, 32768, 128
        return {
            "x": _w((lambda *s: torch.randn(*s, dtype=torch.bfloat16) * 0.05)(B, Sq, D)),
            "k_cache": _w((lambda *s: torch.randn(*s, dtype=torch.bfloat16) * 3.2)(B, H, Sk, Dh)),
            "v_cache": _w((lambda *s: torch.randn(*s, dtype=torch.bfloat16) * 1.6)(B, H, Sk, Dh)),
            "wq": _w((lambda *s: torch.randn(*s, dtype=torch.bfloat16) * 3.2)(D, H * Dh) * (D ** -0.5)),
            "wo": _w((lambda *s: torch.randn(*s, dtype=torch.bfloat16) * 1.6)(H * Dh, D) * ((H * Dh) ** -0.5)),
        }
    _A_in = _A_setup()

    def _B_setup():
        B, S, D, F = 1, 1, 512, 1792
        return {
            "x": _w((lambda *s: torch.randn(*s, dtype=torch.bfloat16) * 0.05)(B, S, D)),
            "w_gate": _w((lambda *s: torch.randn(*s, dtype=torch.bfloat16) * 0.8)(D, F)),
            "w_up": _w((lambda *s: torch.randn(*s, dtype=torch.bfloat16) * 0.05)(D, F)),
            "w_down": _w((lambda *s: torch.randn(*s, dtype=torch.bfloat16) * 0.05)(F, D)),
        }
    _B_in = _B_setup()

    return {
        "A_x": _A_in["x"],
        "A_k_cache": _A_in["k_cache"],
        "A_v_cache": _A_in["v_cache"],
        "A_wq": _A_in["wq"],
        "A_wo": _A_in["wo"],
        "B_x": _B_in["x"],
        "B_w_gate": _B_in["w_gate"],
        "B_w_up": _B_in["w_up"],
        "B_w_down": _B_in["w_down"],
    }

def reference(A_x, A_k_cache, A_v_cache, A_wq, A_wo, B_x, B_w_gate, B_w_up, B_w_down):
    def _A_ref(x, k_cache, v_cache, wq, wo):
        B, Sq, D = x.shape
        H, Dh = 32, 128
        q = (lambda x, *d: x.permute(*d))((x @ wq).reshape(B, Sq, H, Dh), 0, 2, 1, 3)
        scores = torch.einsum("bhqd,bhkd->bhqk", q, k_cache) * (Dh ** -0.5)
        p = _softmax(scores, axis=-1)
        o = torch.einsum("bhqk,bhkd->bhqd", p, v_cache)
        return (lambda x, *d: x.permute(*d))(o, 0, 2, 1, 3).reshape(B, Sq, H * Dh) @ wo

    def _B_ref(x, w_gate, w_up, w_down):
        g = torch.einsum("bsd,df->bsf", x, w_gate)
        u = torch.einsum("bsd,df->bsf", x, w_up)
        h = _sigmoid(g) * u
        return torch.einsum("bsf,fd->bsd", h, w_down)

    x = A_x
    xn = _layernorm(x, axis=-1)
    return x + _A_ref(xn, A_k_cache, A_v_cache, A_wq, A_wo) + _B_ref(xn, B_w_gate, B_w_up, B_w_down)
```

```cpp
#include <hip/hip_runtime.h>
#include <hip/hip_bf16.h>
#include <stdint.h>

typedef unsigned u32x4 __attribute__((ext_vector_type(4)));
typedef float f32x4 __attribute__((ext_vector_type(4)));
typedef float f32x2 __attribute__((ext_vector_type(2)));
typedef __bf16 bf16x2 __attribute__((ext_vector_type(2)));

constexpr int DM = 512, NH = 32, SK = 32768, DH = 128, FF = 1792;
constexpr int NW = 16, NT = NW * 64;
constexpr int NSPLIT = 8;
constexpr int NATT = NH * NSPLIT;
constexpr int KPW = SK / NSPLIT;
constexpr int TK = 16;
constexpr int NTILE = KPW / TK / NW;
constexpr int FCOLS = 64;
constexpr int NFFN = FF / FCOLS;
constexpr int NPART = NATT + NFFN;
constexpr size_t WS_Y = 0, WS_ML = (size_t)NPART * DM * 4;

__device__ __forceinline__ float dot2(unsigned a, unsigned b, float acc) {
  return __builtin_amdgcn_fdot2_f32_bf16(__builtin_bit_cast(bf16x2, a), __builtin_bit_cast(bf16x2, b), acc, false);
}
template <int CTRL> __device__ __forceinline__ float dpp(float x) {
  return __builtin_bit_cast(float, __builtin_amdgcn_mov_dpp(__builtin_bit_cast(int, x), CTRL, 0xf, 0xf, true));
}
constexpr int XOR1 = 0xB1, XOR2 = 0x4E, XOR7 = 0x141, XOR8 = 0x128;
__device__ __forceinline__ float row16_sum(float x) {
  x += dpp<XOR8>(x); x += dpp<XOR7>(x); x += dpp<XOR2>(x); x += dpp<XOR1>(x);
  return x;
}
__device__ __forceinline__ float xrow_sum(float x) {
  auto s = __builtin_amdgcn_permlane16_swap(__float_as_uint(x), __float_as_uint(x), false, false);
  x = __uint_as_float(s[0]) + __uint_as_float(s[1]);
  auto t = __builtin_amdgcn_permlane32_swap(__float_as_uint(x), __float_as_uint(x), false, false);
  return __uint_as_float(t[0]) + __uint_as_float(t[1]);
}
__device__ __forceinline__ float xrow_max(float x) {
  auto s = __builtin_amdgcn_permlane16_swap(__float_as_uint(x), __float_as_uint(x), false, false);
  x = fmaxf(__uint_as_float(s[0]), __uint_as_float(s[1]));
  auto t = __builtin_amdgcn_permlane32_swap(__float_as_uint(x), __float_as_uint(x), false, false);
  return fmaxf(__uint_as_float(t[0]), __uint_as_float(t[1]));
}
__device__ __forceinline__ float bflo(unsigned u) { return __uint_as_float(u << 16); }
__device__ __forceinline__ float bfhi(unsigned u) { return __uint_as_float(u & 0xffff0000u); }
__device__ __forceinline__ float rbf(float x) {
  return __bfloat162float(__float2bfloat16(x));
}
__device__ __forceinline__ unsigned cvtpk(float lo, float hi) {
  bf16x2 r; r[0] = (__bf16)lo; r[1] = (__bf16)hi; return __builtin_bit_cast(unsigned, r);
}
__device__ __forceinline__ u32x4 ldnt(const void* p) { return __builtin_nontemporal_load(reinterpret_cast<const u32x4*>(p)); }
__device__ __forceinline__ u32x4 ld(const void* p) { return *reinterpret_cast<const u32x4*>(p); }

__device__ __forceinline__ void layernorm_wave(const uint16_t* x, float* dst, int lane) {
  u32x4 xv = ld(x + 8 * lane);
  float e[8];
#pragma unroll
  for (int k = 0; k < 4; ++k) { e[2 * k] = bflo(xv[k]); e[2 * k + 1] = bfhi(xv[k]); }
  float s = 0.f;
#pragma unroll
  for (int k = 0; k < 8; ++k) s += e[k];
  s = xrow_sum(row16_sum(s));
  const float mean = s * (1.f / DM);
  float v = 0.f;
#pragma unroll
  for (int k = 0; k < 8; ++k) { const float d = e[k] - mean; v += d * d; }
  v = xrow_sum(row16_sum(v)) * (1.f / DM);
  const float rstd = 1.f / sqrtf(v + 1e-5f);
  f32x4 o0, o1;
#pragma unroll
  for (int k = 0; k < 4; ++k) { o0[k] = rbf((e[k] - mean) * rstd); o1[k] = rbf((e[4 + k] - mean) * rstd); }
  *reinterpret_cast<f32x4*>(dst + 8 * lane) = o0;
  *reinterpret_cast<f32x4*>(dst + 8 * lane + 4) = o1;
}

__global__ void __launch_bounds__(NT, 4) attn_ffn_kernel(const uint16_t* __restrict__ x, const uint16_t* __restrict__ kc, const uint16_t* __restrict__ vc,
                                                         const uint16_t* __restrict__ wq, const uint16_t* __restrict__ wo,
                                                         const uint16_t* __restrict__ wg, const uint16_t* __restrict__ wu, const uint16_t* __restrict__ wd,
                                                         float* __restrict__ Y, float* __restrict__ ML) {
  __shared__ __attribute__((aligned(16))) float lds[NW * DM + 1024];
  float* xn = lds + NW * DM;
  float* qs = lds + NW * DM + 512;
  const int tid = threadIdx.x, lane = tid & 63, wave = __builtin_amdgcn_readfirstlane(tid >> 6);
  const int r = lane >> 4, c = lane & 15;
  const int b = blockIdx.x;

  if (b >= NATT) {
    const int f0 = (b - NATT) * FCOLS;
    const int rs = lane >> 3, cc = lane & 7;
    u32x4 g[4], u[4];
#pragma unroll
    for (int i = 0; i < 4; ++i) {
      const int row = wave * 32 + i * 8 + rs;
      g[i] = ldnt(wg + (size_t)row * FF + f0 + cc * 8);
      u[i] = ldnt(wu + (size_t)row * FF + f0 + cc * 8);
    }
    u32x4 dn[4];
#pragma unroll
    for (int i = 0; i < 4; ++i) dn[i] = ldnt(wd + (size_t)(f0 + wave * 4 + i) * DM + lane * 8);
    if (wave == 0) layernorm_wave(x, xn, lane);
    __syncthreads();
    float ag[8], au[8];
#pragma unroll
    for (int k = 0; k < 8; ++k) { ag[k] = 0.f; au[k] = 0.f; }
#pragma unroll
    for (int i = 0; i < 4; ++i) {
      const float xv = xn[wave * 32 + i * 8 + rs];
#pragma unroll
      for (int k = 0; k < 4; ++k) {
        ag[2 * k] = fmaf(xv, bflo(g[i][k]), ag[2 * k]); ag[2 * k + 1] = fmaf(xv, bfhi(g[i][k]), ag[2 * k + 1]);
        au[2 * k] = fmaf(xv, bflo(u[i][k]), au[2 * k]); au[2 * k + 1] = fmaf(xv, bfhi(u[i][k]), au[2 * k + 1]);
      }
    }
#pragma unroll
    for (int k = 0; k < 8; ++k) {
      ag[k] += dpp<XOR8>(ag[k]); ag[k] = xrow_sum(ag[k]);
      au[k] += dpp<XOR8>(au[k]); au[k] = xrow_sum(au[k]);
    }
    if (lane < 8) {
#pragma unroll
      for (int k = 0; k < 8; ++k) { lds[wave * 128 + cc * 8 + k] = ag[k]; lds[wave * 128 + 64 + cc * 8 + k] = au[k]; }
    }
    __syncthreads();
    if (tid < 64) {
      float gs = 0.f, us = 0.f;
#pragma unroll
      for (int w = 0; w < NW; ++w) { gs += lds[w * 128 + tid]; us += lds[w * 128 + 64 + tid]; }
      gs = rbf(gs); us = rbf(us);
      const float sg = rbf(1.f / (1.f + __expf(-gs)));
      qs[tid] = rbf(sg * us);
    }
    __syncthreads();
    float y[8];
#pragma unroll
    for (int k = 0; k < 8; ++k) y[k] = 0.f;
#pragma unroll
    for (int i = 0; i < 4; ++i) {
      const float hv = qs[wave * 4 + i];
#pragma unroll
      for (int k = 0; k < 4; ++k) { y[2 * k] = fmaf(hv, bflo(dn[i][k]), y[2 * k]); y[2 * k + 1] = fmaf(hv, bfhi(dn[i][k]), y[2 * k + 1]); }
    }
    *reinterpret_cast<f32x4*>(lds + wave * DM + lane * 8) = f32x4{y[0], y[1], y[2], y[3]};
    *reinterpret_cast<f32x4*>(lds + wave * DM + lane * 8 + 4) = f32x4{y[4], y[5], y[6], y[7]};
    __syncthreads();
    if (tid < DM) {
      float s = 0.f;
#pragma unroll
      for (int w = 0; w < NW; ++w) s += lds[w * DM + tid];
      Y[(size_t)b * DM + tid] = s;
    }
    return;
  }

  const int xcd = b & 7, idx = b >> 3;
  const int head = xcd * (NH / 8) + idx / NSPLIT, split = idx % NSPLIT;
  u32x4 wqv[8];
#pragma unroll
  for (int i = 0; i < 8; ++i) wqv[i] = ld(wq + (size_t)(wave * 32 + i * 4 + r) * (NH * DH) + head * DH + c * 8);
  const uint16_t* kb = kc + ((size_t)head * SK + (size_t)split * KPW + wave * TK + r) * DH + c * 8;
  const uint16_t* vb = vc + ((size_t)head * SK + (size_t)split * KPW + wave * TK + r) * DH + c * 8;
  constexpr size_t TSTRIDE = (size_t)NW * TK * DH;
  u32x4 KA[4], VA[4], KB[4], VB[4];
#define ISSUE(Kb, Vb, t_) do { _Pragma("unroll") for (int i_ = 0; i_ < 4; ++i_) Kb[i_] = ldnt(kb + (size_t)(t_) * TSTRIDE + i_ * 4 * DH); \
    _Pragma("unroll") for (int i_ = 0; i_ < 4; ++i_) Vb[i_] = ldnt(vb + (size_t)(t_) * TSTRIDE + i_ * 4 * DH); } while (0)
  if (wave == 0) layernorm_wave(x, xn, lane);
  ISSUE(KA, VA, 0);
  ISSUE(KB, VB, 1);
  __syncthreads();
  {
    float qa[8];
#pragma unroll
    for (int k = 0; k < 8; ++k) qa[k] = 0.f;
#pragma unroll
    for (int i = 0; i < 8; ++i) {
      const float xv = xn[wave * 32 + i * 4 + r];
#pragma unroll
      for (int k = 0; k < 4; ++k) { qa[2 * k] = fmaf(xv, bflo(wqv[i][k]), qa[2 * k]); qa[2 * k + 1] = fmaf(xv, bfhi(wqv[i][k]), qa[2 * k + 1]); }
    }
#pragma unroll
    for (int k = 0; k < 8; ++k) qa[k] = xrow_sum(qa[k]);
    if (r == 0) {
      *reinterpret_cast<f32x4*>(lds + wave * DH + c * 8) = f32x4{qa[0], qa[1], qa[2], qa[3]};
      *reinterpret_cast<f32x4*>(lds + wave * DH + c * 8 + 4) = f32x4{qa[4], qa[5], qa[6], qa[7]};
    }
  }
  __syncthreads();
  if (tid < DH) {
    float s = 0.f;
#pragma unroll
    for (int w = 0; w < NW; ++w) s += lds[w * DH + tid];
    qs[tid] = s;
  }
  __syncthreads();
  u32x4 qpk;
  {
    const f32x4 q0 = *reinterpret_cast<const f32x4*>(qs + c * 8), q1 = *reinterpret_cast<const f32x4*>(qs + c * 8 + 4);
    qpk[0] = cvtpk(q0[0], q0[1]); qpk[1] = cvtpk(q0[2], q0[3]); qpk[2] = cvtpk(q1[0], q1[1]); qpk[3] = cvtpk(q1[2], q1[3]);
  }
  constexpr float C2 = 0.088388347648318440f * 1.4426950408889634f;
  float m = -INFINITY, l = 0.f, acc[8];
#pragma unroll
  for (int k = 0; k < 8; ++k) acc[k] = 0.f;
#define COMPUTE(Kb, Vb) do { float s_[4]; \
    _Pragma("unroll") for (int i_ = 0; i_ < 4; ++i_) { float d_ = dot2(Kb[i_][0], qpk[0], 0.f); d_ = dot2(Kb[i_][1], qpk[1], d_); d_ = dot2(Kb[i_][2], qpk[2], d_); d_ = dot2(Kb[i_][3], qpk[3], d_); \
      s_[i_] = row16_sum(d_) * C2; } \
    const float mn_ = fmaxf(fmaxf(fmaxf(s_[0], s_[1]), fmaxf(s_[2], s_[3])), m); \
    const float al_ = __builtin_amdgcn_exp2f(m - mn_); m = mn_; \
    float p_[4]; _Pragma("unroll") for (int i_ = 0; i_ < 4; ++i_) p_[i_] = __builtin_amdgcn_exp2f(s_[i_] - mn_); \
    l = fmaf(l, al_, (p_[0] + p_[1]) + (p_[2] + p_[3])); \
    _Pragma("unroll") for (int k_ = 0; k_ < 8; ++k_) acc[k_] *= al_; \
    _Pragma("unroll") for (int i_ = 0; i_ < 4; ++i_) _Pragma("unroll") for (int k_ = 0; k_ < 4; ++k_) { \
      acc[2 * k_] = fmaf(p_[i_], bflo(Vb[i_][k_]), acc[2 * k_]); acc[2 * k_ + 1] = fmaf(p_[i_], bfhi(Vb[i_][k_]), acc[2 * k_ + 1]); } } while (0)
#pragma unroll 1
  for (int t = 0; t < NTILE - 2; t += 2) {
    COMPUTE(KA, VA);
    ISSUE(KA, VA, t + 2);
    COMPUTE(KB, VB);
    ISSUE(KB, VB, t + 3);
  }
  u32x4 wov[8];
#pragma unroll
  for (int i = 0; i < 8; ++i) wov[i] = ld(wo + (size_t)(head * DH + wave * 8 + i) * DM + lane * 8);
  COMPUTE(KA, VA);
  COMPUTE(KB, VB);
  {
    const float mw = xrow_max(m);
    const float sc = __builtin_amdgcn_exp2f(m - mw);
    l = xrow_sum(l * sc);
#pragma unroll
    for (int k = 0; k < 8; ++k) acc[k] = xrow_sum(acc[k] * sc);
    float* rec = lds + wave * 132;
    if (r == 0) {
      *reinterpret_cast<f32x4*>(rec + 4 + c * 8) = f32x4{acc[0], acc[1], acc[2], acc[3]};
      *reinterpret_cast<f32x4*>(rec + 4 + c * 8 + 4) = f32x4{acc[4], acc[5], acc[6], acc[7]};
      if (c == 0) { rec[0] = mw; rec[1] = l; }
    }
  }
  __syncthreads();
  float M = -INFINITY;
#pragma unroll
  for (int w = 0; w < NW; ++w) M = fmaxf(M, lds[w * 132]);
  float L = 0.f, a[8];
#pragma unroll
  for (int k = 0; k < 8; ++k) a[k] = 0.f;
#pragma unroll
  for (int w = 0; w < NW; ++w) {
    const float* rec = lds + w * 132;
    const float sc = __builtin_amdgcn_exp2f(rec[0] - M);
    L = fmaf(sc, rec[1], L);
    const f32x4 a0 = *reinterpret_cast<const f32x4*>(rec + 4 + wave * 8), a1 = *reinterpret_cast<const f32x4*>(rec + 4 + wave * 8 + 4);
#pragma unroll
    for (int k = 0; k < 4; ++k) { a[k] = fmaf(sc, a0[k], a[k]); a[4 + k] = fmaf(sc, a1[k], a[4 + k]); }
  }
  float y[8];
#pragma unroll
  for (int k = 0; k < 8; ++k) y[k] = 0.f;
#pragma unroll
  for (int i = 0; i < 8; ++i)
#pragma unroll
    for (int k = 0; k < 4; ++k) { y[2 * k] = fmaf(a[i], bflo(wov[i][k]), y[2 * k]); y[2 * k + 1] = fmaf(a[i], bfhi(wov[i][k]), y[2 * k + 1]); }
  __syncthreads();
  *reinterpret_cast<f32x4*>(lds + wave * DM + lane * 8) = f32x4{y[0], y[1], y[2], y[3]};
  *reinterpret_cast<f32x4*>(lds + wave * DM + lane * 8 + 4) = f32x4{y[4], y[5], y[6], y[7]};
  __syncthreads();
  const int part = head * NSPLIT + split;
  if (tid < DM) {
    float s = 0.f;
#pragma unroll
    for (int w = 0; w < NW; ++w) s += lds[w * DM + tid];
    Y[(size_t)part * DM + tid] = s;
  }
  if (tid == 0) { ML[2 * part] = M; ML[2 * part + 1] = L; }
}

constexpr int CCOLS = 32;
__global__ void __launch_bounds__(1024) combine_kernel(const uint16_t* __restrict__ x, const float* __restrict__ Y, const float* __restrict__ ML,
                                                       uint16_t* __restrict__ out) {
  __shared__ __attribute__((aligned(16))) float red[128 * CCOLS];
  __shared__ float red2[2][8 * CCOLS];
  const int tid = threadIdx.x, cq = tid & 7, p0 = tid >> 3;
  const int col = blockIdx.x * CCOLS + cq * 4;
  f32x4 yv[2], yf = f32x4{0.f, 0.f, 0.f, 0.f};
#pragma unroll
  for (int i = 0; i < 2; ++i) yv[i] = *reinterpret_cast<const f32x4*>(Y + (size_t)(p0 + 128 * i) * DM + col);
  if (p0 < NFFN) yf = *reinterpret_cast<const f32x4*>(Y + (size_t)(NATT + p0) * DM + col);
  f32x4 s = f32x4{0.f, 0.f, 0.f, 0.f};
#pragma unroll
  for (int i = 0; i < 2; ++i) {
    const int p = p0 + 128 * i, h = p / NSPLIT;
    float M = -INFINITY;
    f32x2 ml[NSPLIT];
#pragma unroll
    for (int j = 0; j < NSPLIT; ++j) { ml[j] = *reinterpret_cast<const f32x2*>(ML + 2 * (h * NSPLIT + j)); M = fmaxf(M, ml[j][0]); }
    float L = 0.f;
#pragma unroll
    for (int j = 0; j < NSPLIT; ++j) L = fmaf(__builtin_amdgcn_exp2f(ml[j][0] - M), ml[j][1], L);
    const float w = __builtin_amdgcn_exp2f(ML[2 * p] - M) / L;
    s += yv[i] * w;
  }
  *reinterpret_cast<f32x4*>(red + p0 * CCOLS + cq * 4) = s;
  __syncthreads();
  if (tid < 256) {
    const int cc = tid & 31, g = tid >> 5;
    float t = 0.f;
#pragma unroll
    for (int j = 0; j < 16; ++j) t += red[(g * 16 + j) * CCOLS + cc];
    red2[0][g * CCOLS + cc] = t;
  }
  __syncthreads();
  *reinterpret_cast<f32x4*>(red + p0 * CCOLS + cq * 4) = yf;
  __syncthreads();
  if (tid < 32) {
    float ta = 0.f, tf = 0.f;
#pragma unroll
    for (int g = 0; g < 8; ++g) ta += red2[0][g * CCOLS + tid];
#pragma unroll
    for (int j = 0; j < NFFN; ++j) tf += red[j * CCOLS + tid];
    const int cidx = blockIdx.x * CCOLS + tid;
    const float xv = __uint_as_float((unsigned)x[cidx] << 16);
    const float o = rbf(xv + rbf(ta)) + rbf(tf);
    __hip_bfloat16 ob = __float2bfloat16(o);
    out[cidx] = *reinterpret_cast<uint16_t*>(&ob);
  }
}

extern "C" void kernel_launch(void* const* d_in, const int* in_sizes, int n_in, void* d_out, int out_size, void* d_ws, size_t ws_size,
                              hipStream_t stream) {
  const uint16_t* x = (const uint16_t*)d_in[0];
  const uint16_t* kc = (const uint16_t*)d_in[1];
  const uint16_t* vc = (const uint16_t*)d_in[2];
  const uint16_t* wq = (const uint16_t*)d_in[3];
  const uint16_t* wo = (const uint16_t*)d_in[4];
  const uint16_t* wg = (const uint16_t*)d_in[6];
  const uint16_t* wu = (const uint16_t*)d_in[7];
  const uint16_t* wd = (const uint16_t*)d_in[8];
  float* Y = (float*)((char*)d_ws + WS_Y);
  float* ML = (float*)((char*)d_ws + WS_ML);
  attn_ffn_kernel<<<NPART, NT, 0, stream>>>(x, kc, vc, wq, wo, wg, wu, wd, Y, ML);
  combine_kernel<<<DM / CCOLS, 1024, 0, stream>>>(x, Y, ML, (uint16_t*)d_out);
}
```

```cpp
#include <hip/hip_runtime.h>
#include <hip/hip_bf16.h>
#include <stdint.h>
#include <utility>

#define STAMPS 0
#define PH 3
#define ROTB 32768
#define QSLP1 8
#define QSLP2 20
#define RSLP 1
#define JWO 2
#define JTR 20
#define JFF 10
#define JLS 23

typedef unsigned u32x4 __attribute__((ext_vector_type(4)));
typedef unsigned u32x2 __attribute__((ext_vector_type(2)));
typedef float f32x4 __attribute__((ext_vector_type(4)));
typedef float f32x2 __attribute__((ext_vector_type(2)));
typedef short s16x4 __attribute__((ext_vector_type(4)));
typedef __bf16 bf16x2 __attribute__((ext_vector_type(2)));
typedef __bf16 bf16x8 __attribute__((ext_vector_type(8)));

constexpr int DM = 512, NH = 32, DH = 128, FF = 1792;
constexpr int NW = 16, NT = NW * 64;
constexpr int NWG = 256;
constexpr int U = 4;
constexpr int CHUNK = 1 << 20;
constexpr int TILEB = NW * U * 1024;
constexpr int NQ = CHUNK / TILEB;
constexpr int NRED = 16;
constexpr unsigned SPIN_CAP = 1u << 22;
constexpr size_t WS_E = 0, WS_S = 2048, WS_Q = 4096, WS_H = WS_Q + NH * 2048, WS_Y = WS_H + 4 * FF * 8, WS_ST = 64ull << 20;

__device__ __forceinline__ float dot2(unsigned a, unsigned b, float acc) {
  return __builtin_amdgcn_fdot2_f32_bf16(__builtin_bit_cast(bf16x2, a), __builtin_bit_cast(bf16x2, b), acc, false);
}
template <int CTRL> __device__ __forceinline__ float dpp(float x) {
  return __builtin_bit_cast(float, __builtin_amdgcn_mov_dpp(__builtin_bit_cast(int, x), CTRL, 0xf, 0xf, true));
}
constexpr int XOR1 = 0xB1, XOR2 = 0x4E, XOR7 = 0x141, XOR8 = 0x128, ROR4 = 0x124;
__device__ __forceinline__ float row16_sum(float x) { x += dpp<XOR8>(x); x += dpp<XOR7>(x); x += dpp<XOR2>(x); x += dpp<XOR1>(x); return x; }
__device__ __forceinline__ float row16_max(float x) { x = fmaxf(x, dpp<XOR8>(x)); x = fmaxf(x, dpp<XOR7>(x)); x = fmaxf(x, dpp<XOR2>(x)); x = fmaxf(x, dpp<XOR1>(x)); return x; }
__device__ __forceinline__ float sw16_sum(float a, float b) {
  auto s = __builtin_amdgcn_permlane16_swap(__float_as_uint(a), __float_as_uint(b), false, false);
  return __uint_as_float(s[0]) + __uint_as_float(s[1]);
}
__device__ __forceinline__ float sw32_sum(float a, float b) {
  auto t = __builtin_amdgcn_permlane32_swap(__float_as_uint(a), __float_as_uint(b), false, false);
  return __uint_as_float(t[0]) + __uint_as_float(t[1]);
}
__device__ __forceinline__ float x16_sum(float x) { return sw16_sum(x, x); }
__device__ __forceinline__ float x32_sum(float x) { return sw32_sum(x, x); }
__device__ __forceinline__ float xrow_sum(float x) { return x32_sum(x16_sum(x)); }
__device__ __forceinline__ float xrow_max(float x) {
  auto s = __builtin_amdgcn_permlane16_swap(__float_as_uint(x), __float_as_uint(x), false, false);
  x = fmaxf(__uint_as_float(s[0]), __uint_as_float(s[1]));
  auto t = __builtin_amdgcn_permlane32_swap(__float_as_uint(x), __float_as_uint(x), false, false);
  return fmaxf(__uint_as_float(t[0]), __uint_as_float(t[1]));
}
__device__ __forceinline__ float bflo(unsigned u) { return __uint_as_float(u << 16); }
__device__ __forceinline__ float bfhi(unsigned u) { return __uint_as_float(u & 0xffff0000u); }
__device__ __forceinline__ unsigned cvtpk(float lo, float hi) { bf16x2 r; r[0] = (__bf16)lo; r[1] = (__bf16)hi; return __builtin_bit_cast(unsigned, r); }
__device__ __forceinline__ float rbf(float x) { return (float)(__bf16)x; }
__device__ __forceinline__ f32x4 mfma(u32x4 a, u32x4 b, f32x4 c) {
  return __builtin_amdgcn_mfma_f32_16x16x32_bf16(__builtin_bit_cast(bf16x8, a), __builtin_bit_cast(bf16x8, b), c, 0, 0, 0);
}
__device__ __forceinline__ unsigned pair_lo(unsigned a, unsigned b) { return __builtin_amdgcn_perm(b, a, 0x05040100u); }
__device__ __forceinline__ unsigned pair_hi(unsigned a, unsigned b) { return __builtin_amdgcn_perm(b, a, 0x07060302u); }
__device__ __forceinline__ f32x2 reduce16(const float (&y)[8]) {
  float z[4]; f32x2 t;
#pragma unroll
  for (int j = 0; j < 4; ++j) z[j] = sw32_sum(y[j], y[j + 4]);
#pragma unroll
  for (int j = 0; j < 2; ++j) { float v = sw16_sum(z[j], z[j + 2]); v += dpp<XOR8>(v); v += dpp<ROR4>(v); t[j] = v; }
  return t;
}
__device__ __forceinline__ u32x2 tr_read(const unsigned* p) {
  return __builtin_bit_cast(u32x2, __builtin_amdgcn_ds_read_tr16_b64_v4i16((__attribute__((address_space(3))) s16x4*)p));
}

#define GLD(dst, voff, sbase, OFF, POL) asm volatile("global_load_dwordx4 %0, %1, %2 offset:%3 " POL : "=v"(dst) : "v"(voff), "s"(sbase), "i"(OFF) : "memory")
#define GLD2(dst, voff, sbase, POL) asm volatile("global_load_dwordx2 %0, %1, %2 " POL : "=v"(dst) : "v"(voff), "s"(sbase) : "memory")
#define WAITVM(N) asm volatile("s_waitcnt vmcnt(%0)" :: "i"(N) : "memory")
#define WAITLDS() asm volatile("s_waitcnt lgkmcnt(0)" ::: "memory")
#define PIN(x) asm volatile("" : "+v"(x))
#define BARRIER() do { WAITLDS(); __builtin_amdgcn_s_barrier(); asm volatile("" ::: "memory"); } while (0)
__device__ __forceinline__ void st16(const char* sbase, unsigned voff, u32x4 v) {
  asm volatile("global_store_dwordx4 %0, %1, %2 sc1\n\ts_nop 1" :: "v"(voff), "v"(v), "s"(sbase) : "memory");
}
__device__ __forceinline__ void st8(const char* sbase, unsigned voff, u32x2 v) {
  asm volatile("global_store_dwordx2 %0, %1, %2 sc1\n\ts_nop 1" :: "v"(voff), "v"(v), "s"(sbase) : "memory");
}

constexpr int L_Q = 0;
constexpr int L_TAG = L_Q + 64;
constexpr int L_FP = L_TAG + 4;
constexpr int L_M = L_FP + 256;
constexpr int L_L = L_M + 16;
constexpr int L_REC = L_L + 16;
constexpr int L_RA = L_REC + 16 * 68;
constexpr int L_TR = L_RA + 512;
constexpr int L_X = L_TR + 16 * 512;
constexpr int L_END = L_X + 256;

constexpr bool reissue(int j) { return j + 2 >= PH && j + 2 < 2 * NQ; }
constexpr bool wostep(int j) { return j >= JWO && j < JWO + 8; }
constexpr int younger(int j) {
  int n = 0, pos = -1;
  for (int i = 0; i < PH; ++i) { n += U; if (i == j) pos = n; }
  for (int i = 0; i < j; ++i) {
    if (reissue(i)) { n += U; if (i + 2 == j) pos = n; }
    if (wostep(i)) n += 1;
  }
  return n - pos;
}

template <typename F, int... I> __device__ __forceinline__ void static_for_impl(F&& f, std::integer_sequence<int, I...>) { (f(std::integral_constant<int, I>{}), ...); }
template <int N, typename F> __device__ __forceinline__ void static_for(F&& f) { static_for_impl(f, std::make_integer_sequence<int, N>{}); }

__global__ void __launch_bounds__(NT, 4) fused_kernel(const char* __restrict__ x, const char* __restrict__ kc, const char* __restrict__ vc,
                                                      const char* __restrict__ wq, const char* __restrict__ wo,
                                                      const char* __restrict__ wg, const char* __restrict__ wu, const char* __restrict__ wd,
                                                      char* __restrict__ ws, char* __restrict__ out) {
  __shared__ __attribute__((aligned(16))) unsigned ldsu[L_END];
  float* lds = reinterpret_cast<float*>(ldsu);
  const int tid = threadIdx.x, lane = tid & 63, wave = __builtin_amdgcn_readfirstlane(tid >> 6);
  const int r = lane >> 4, c = lane & 15;
  const int b = blockIdx.x;
  const int xcd = b & 7, idx = b >> 3;
  const int cid = xcd * 32 + idx;
  const int head = cid >> 3, split = cid & 7;
  const unsigned rowsub = lane >> 2, cq = lane & 3;
  unsigned tagw = 0;

  if (wave == 0 || (wave == 1 && idx < 28)) {
    __builtin_amdgcn_s_setprio(3);
    const char* wp; const char* pub; unsigned rstride; int rh;
    if (wave == 0) { rstride = NH * DH * 2; rh = split >> 2; wp = wq + head * (DH * 2) + (split & 3) * 64; pub = ws + WS_Q + head * 2048 + rh * 1024 + (split & 3) * 256; }
    else { const int cg = xcd * 7 + idx % 7, mr = idx / 7;
      rstride = FF * 2; rh = mr & 1; wp = ((mr >> 1) ? wu : wg) + cg * 64; pub = ws + WS_H + mr * (FF * 8) + cg * 256; }
    wp += (size_t)(rh * 256) * rstride;
    const unsigned wvo = rowsub * 16 * rstride + cq * 16;
    u32x4 xf, xa, xb, wv[16];
    unsigned ep;
    GLD(xf, lane * 16, x, 0, "");
    GLD(xa, rowsub * 32, x + rh * 512, 0, ""); GLD(xb, rowsub * 32, x + rh * 512, 16, "");
    asm volatile("global_load_dword %0, %1, %2 sc1" : "=v"(ep) : "v"(0), "s"(ws + WS_E) : "memory");
#pragma unroll
    for (int i = 0; i < 16; ++i) GLD(wv[i], wvo, wp + (size_t)i * rstride, 0, "nt");
    if (wave == 0 && lane == 0) ldsu[L_TAG + 1] = 0u;
    BARRIER();
    WAITVM(16); PIN(xf); PIN(xa); PIN(xb); PIN(ep);
    if (wave == 0) *reinterpret_cast<u32x4*>(ldsu + L_X + lane * 4) = xf;
    unsigned xn[8];
    {
      float e[8];
#pragma unroll
      for (int k = 0; k < 4; ++k) { e[2 * k] = bflo(xf[k]); e[2 * k + 1] = bfhi(xf[k]); }
      float s = ((e[0] + e[1]) + (e[2] + e[3])) + ((e[4] + e[5]) + (e[6] + e[7]));
      s = xrow_sum(row16_sum(s));
      const float mean = s * (1.f / DM);
      float v = 0.f;
#pragma unroll
      for (int k = 0; k < 8; ++k) { const float d = e[k] - mean; v = fmaf(d, d, v); }
      v = xrow_sum(row16_sum(v)) * (1.f / DM);
      const float mean_b = rbf(mean), inv = 1.f / rbf(sqrtf(rbf(rbf(v) + 1e-5f)));
#pragma unroll
      for (int k = 0; k < 4; ++k) {
        xn[k] = cvtpk(rbf(bflo(xa[k]) - mean_b) * inv, rbf(bfhi(xa[k]) - mean_b) * inv);
        xn[4 + k] = cvtpk(rbf(bflo(xb[k]) - mean_b) * inv, rbf(bfhi(xb[k]) - mean_b) * inv);
      }
#pragma unroll
      for (int k = 0; k < 8; ++k) PIN(xn[k]);
    }
    tagw = ep + 1u;
    WAITVM(0);
#pragma unroll
    for (int i = 0; i < 16; ++i) PIN(wv[i]);
    float a[8];
#pragma unroll
    for (int k = 0; k < 8; ++k) a[k] = 0.f;
#pragma unroll
    for (int i = 0; i < 8; ++i)
#pragma unroll
      for (int k = 0; k < 4; ++k) {
        a[2 * k] = dot2(pair_lo(wv[2 * i][k], wv[2 * i + 1][k]), xn[i], a[2 * k]);
        a[2 * k + 1] = dot2(pair_hi(wv[2 * i][k], wv[2 * i + 1][k]), xn[i], a[2 * k + 1]);
      }
    const f32x2 t = reduce16(a);
    if (c < 4) st16(pub, (c * 4 + r) * 16, u32x4{__float_as_uint(t[0]), tagw, __float_as_uint(t[1]), tagw});
    if (wave != 0) __builtin_amdgcn_s_setprio(0);
  } else {
    __builtin_amdgcn_s_barrier();
  }

  const char* kb = kc + (size_t)cid * CHUNK;
  const char* vb = vc + (size_t)cid * CHUNK;
  const unsigned kvo = lane * 16;
  const unsigned rot0 = (unsigned)cid * ROTB + wave * (U * 1024);
  u32x4 KV[2 * NQ][U];
#define ISSUE(j_) do { const char* p_ = ((j_) >= NQ ? vb : kb) + ((((j_) % NQ) * TILEB + rot0) & (CHUNK - 1)); \
    GLD(KV[j_][0], kvo, p_, 0, "nt"); GLD(KV[j_][1], kvo, p_, 1024, "nt"); GLD(KV[j_][2], kvo, p_, 2048, "nt"); GLD(KV[j_][3], kvo, p_, 3072, "nt"); } while (0)
#pragma unroll
  for (int j = 0; j < PH; ++j) ISSUE(j);
  if (wave == 0) {
    const char* qb = ws + WS_Q + head * 2048;
    u32x4 pa[2], pb[2];
    __builtin_amdgcn_s_sleep(QSLP1);
    GLD(pa[0], lane * 16, qb, 0, "sc1"); GLD(pa[1], lane * 16, qb, 1024, "sc1");
    __builtin_amdgcn_s_sleep(QSLP2);
    GLD(pb[0], lane * 16, qb, 0, "sc1"); GLD(pb[1], lane * 16, qb, 1024, "sc1");
    WAITVM(2); PIN(pa[0]); PIN(pa[1]);
#define QOK(p) __all(p[0][1] == tagw && p[0][3] == tagw && p[1][1] == tagw && p[1][3] == tagw)
    if (!QOK(pa)) {
      WAITVM(0);
#pragma unroll
      for (int j = 0; j < 2; ++j)
        asm volatile("v_mov_b32 %0, %4\n\tv_mov_b32 %1, %5\n\tv_mov_b32 %2, %6\n\tv_mov_b32 %3, %7"
                     : "=&v"(pa[j][0]), "=&v"(pa[j][1]), "=&v"(pa[j][2]), "=&v"(pa[j][3]) : "v"(pb[j][0]), "v"(pb[j][1]), "v"(pb[j][2]), "v"(pb[j][3]));
      for (unsigned spins = 0; !QOK(pa) && spins < SPIN_CAP; ++spins) {
        GLD(pa[0], lane * 16, qb, 0, "sc1"); GLD(pa[1], lane * 16, qb, 1024, "sc1");
        WAITVM(0); PIN(pa[0]); PIN(pa[1]);
      }
    }
    ldsu[L_Q + lane] = cvtpk(__uint_as_float(pa[0][0]) + __uint_as_float(pa[1][0]), __uint_as_float(pa[0][2]) + __uint_as_float(pa[1][2]));
    if (lane == 0) ldsu[L_TAG] = tagw;
    WAITLDS();
    if (lane == 0) __hip_atomic_store(&ldsu[L_TAG + 1], 1u, __ATOMIC_RELAXED, __HIP_MEMORY_SCOPE_WORKGROUP);
    asm volatile("s_waitcnt vmcnt(0)" :: "v"(pb[0]), "v"(pb[1]) : "memory");
    __builtin_amdgcn_s_setprio(0);
  }
  u32x4 mE, mO;
#pragma unroll
  for (int j = 0; j < 4; ++j) {
    const unsigned mj = (((c & 7) >> 1) == j) ? ((c & 1) ? 0xffff0000u : 0x0000ffffu) : 0u;
    mE[j] = c < 8 ? mj : 0u; mO[j] = c < 8 ? 0u : mj;
  }
  while (__hip_atomic_load(&ldsu[L_TAG + 1], __ATOMIC_RELAXED, __HIP_MEMORY_SCOPE_WORKGROUP) == 0u) __builtin_amdgcn_s_sleep(2);
  asm volatile("" ::: "memory");
  const u32x4 qpk = *reinterpret_cast<const u32x4*>(ldsu + L_Q + c * 4);
  const unsigned tag = ldsu[L_TAG];

  constexpr float SCALE = 0.088388347648318440f, LOG2E = 1.4426950408889634f;
  unsigned sp[NQ];
  float mx = -INFINITY, Mw = 0.f, fsum = 0.f, fw = 0.f;
  f32x4 acc = {0.f, 0.f, 0.f, 0.f};
  u32x4 wov[8], wdv;
  u32x2 hv, sv;
  const bool hi8 = c >= 8;
  const int cj = xcd * 2 + (idx & 1), kj = idx >> 1;
  const unsigned colo = rowsub * (DM * 2) + cq * 16;
  const unsigned hvo = cq * (FF * 8) + rowsub * 8;
  unsigned recw, recr, yoff;

  static_for<2 * NQ>([&](auto jc_) __attribute__((always_inline)) {
    constexpr int j = decltype(jc_)::value;
    constexpr int WJ = younger(j);
    if constexpr (j < NQ) {
      float dd[4], t[2];
      WAITVM(WJ);
#pragma unroll
      for (int i = 0; i < 4; ++i) {
        PIN(KV[j][i]);
        dd[i] = dot2(KV[j][i][0], qpk[0], 0.f);
#pragma unroll
        for (int k = 1; k < 4; ++k) dd[i] = dot2(KV[j][i][k], qpk[k], dd[i]);
      }
#pragma unroll
      for (int i = 0; i < 4; ++i) PIN(dd[i]);
      if constexpr (reissue(j)) ISSUE(j + 2);
#pragma unroll
      for (int i = 0; i < 2; ++i) {
        t[i] = hi8 ? dd[2 * i + 1] : dd[2 * i]; const float o = hi8 ? dd[2 * i] : dd[2 * i + 1];
        t[i] += dpp<XOR8>(o); t[i] += dpp<XOR7>(t[i]); t[i] += dpp<XOR2>(t[i]); t[i] += dpp<XOR1>(t[i]);
      }
      const unsigned raw = cvtpk(t[0], t[1]);
      sp[j] = cvtpk(bflo(raw) * SCALE, bfhi(raw) * SCALE);
      mx = fmaxf(mx, fmaxf(bflo(sp[j]), bfhi(sp[j])));
      PIN(sp[j]); PIN(mx);
      if constexpr (j == NQ - 1) {
        mx = xrow_max(fmaxf(mx, dpp<XOR8>(mx)));
        if (lane == 0) lds[L_M + wave] = mx;
      }
    } else {
      if constexpr (j == NQ) {
        Mw = row16_max(lds[L_M + c]) * LOG2E;
        float l = 0.f;
#pragma unroll
        for (int i = 0; i < NQ; ++i) {
          const float p0 = __builtin_amdgcn_exp2f(fmaf(bflo(sp[i]), LOG2E, -Mw)), p1 = __builtin_amdgcn_exp2f(fmaf(bfhi(sp[i]), LOG2E, -Mw));
          l += p0 + p1;
          sp[i] = cvtpk(p0, p1);
          PIN(sp[i]);
        }
        l = xrow_sum(l + dpp<XOR8>(l));
        if (lane == 0) lds[L_L + wave] = l;
      }
      const unsigned pp0 = pair_lo(sp[j - NQ], sp[j - NQ]), pp1 = pair_hi(sp[j - NQ], sp[j - NQ]);
      u32x4 a0 = mE & pp0, a1 = mO & pp0, a2 = mE & pp1, a3 = mO & pp1;
      if constexpr (j == 2 * NQ - 1) {
        fw = lds[L_TAG + 2];
        recw = (L_REC + wave * 68 + c * 4 + (r & 1) * 2 + (r >> 1)) * 4; recr = (L_REC + c * 68 + r * 4) * 4;
        yoff = (unsigned)(tid >> 6) * (NWG * 128) + (lane >> 5) * 64 + c * 4;
        PIN(fw); PIN(recw); PIN(recr); PIN(yoff); PIN(a0); PIN(a1); PIN(a2); PIN(a3);
      }
      WAITVM(WJ);
#pragma unroll
      for (int i = 0; i < 4; ++i) PIN(KV[j][i]);
      acc = mfma(a0, KV[j][0], acc); acc = mfma(a1, KV[j][1], acc); acc = mfma(a2, KV[j][2], acc); acc = mfma(a3, KV[j][3], acc);
      PIN(acc);
      if constexpr (reissue(j)) ISSUE(j + 2);
    }
    if constexpr (wostep(j)) GLD(wov[j - JWO], colo + (tid >> 6) * 64, wo + (size_t)(head * DH + (j - JWO) * 16) * (DM * 2), 0, "nt");
    if constexpr (j < 2 * NQ - 1) BARRIER();
    if constexpr (j == JFF) {
      if (wave < 7) {
        GLD(wdv, colo, wd + (size_t)(kj * 112 + wave * 16) * (DM * 2) + cj * 64, 0, "nt");
        GLD2(hv, hvo, ws + WS_H + (kj * 112 + wave * 16) * 8, "sc1");
      }
    }
    if constexpr (j == JFF + 3) {
      if (wave < 7) {
        PIN(wdv); PIN(hv);
        for (unsigned spins = 0; !__all(hv[1] == tag) && spins < SPIN_CAP; ++spins) {
          __builtin_amdgcn_s_sleep(8);
          GLD2(hv, hvo, ws + WS_H + (kj * 112 + wave * 16) * 8, "sc1");
          WAITVM(0); PIN(hv);
        }
        float gu = __uint_as_float(hv[0]); gu += dpp<XOR1>(gu);
        const float gs = rbf(dpp<0x00>(gu)), us = rbf(dpp<0xAA>(gu));
        const float h = rbf(rbf(1.f / (1.f + __expf(-gs))) * us);
        float y[8];
#pragma unroll
        for (int k = 0; k < 4; ++k) { y[2 * k] = h * bflo(wdv[k]); y[2 * k + 1] = h * bfhi(wdv[k]); }
        const f32x2 f2 = reduce16(y);
        if (c < 4) *reinterpret_cast<f32x2*>(lds + L_FP + wave * 32 + c * 8 + r * 2) = f2;
      }
    }
    if constexpr (j == JFF + 4) {
      if (wave == cj) {
#pragma unroll
        for (int w = 0; w < 7; ++w) fsum += lds[L_FP + w * 32 + (lane >> 5) * 16 + c];
      }
      PIN(fsum);
    }
    if constexpr (j == NQ) {
      if (wave == 0) {
        const float lse = Mw + __builtin_amdgcn_logf(row16_sum(lds[L_L + c]));
        if (lane == 0) st8(ws + WS_S + cid * 8, 0, u32x2{__float_as_uint(lse), tag});
      }
    }
    if constexpr (j == JLS) {
      if (wave == 0) GLD2(sv, (lane & 7) * 8, ws + WS_S + head * 64, "sc1");
    }
    if constexpr (j == JLS + 3) {
      if (wave == 0) {
        PIN(sv);
        for (unsigned spins = 0; !__all(sv[1] == tag) && spins < SPIN_CAP; ++spins) {
          __builtin_amdgcn_s_sleep(8);
          GLD2(sv, (lane & 7) * 8, ws + WS_S + head * 64, "sc1");
          WAITVM(0); PIN(sv);
        }
        const float ls = __uint_as_float(sv[0]);
        float S = fmaxf(ls, dpp<XOR1>(ls)); S = fmaxf(S, dpp<XOR2>(S)); S = fmaxf(S, dpp<XOR7>(S));
        float e = __builtin_amdgcn_exp2f(ls - S); e += dpp<XOR1>(e); e += dpp<XOR2>(e); e += dpp<XOR7>(e);
        if (lane == 0) lds[L_TAG + 2] = __builtin_amdgcn_exp2f(Mw - S) / e;
      }
    }
    if constexpr (j == JTR) {
      unsigned* tb = ldsu + L_TR + wave * 512;
      const unsigned* tr = tb + (r * 8 + ((lane >> 2) & 3)) * 16 + (lane & 3) * 2;
#pragma unroll
      for (int kk = 0; kk < 4; ++kk) {
        PIN(wov[2 * kk]); PIN(wov[2 * kk + 1]);
        *reinterpret_cast<u32x4*>(tb + lane * 4) = wov[2 * kk];
        *reinterpret_cast<u32x4*>(tb + 256 + lane * 4) = wov[2 * kk + 1];
        asm volatile("" ::: "memory");
#pragma unroll
        for (int nt = 0; nt < 2; ++nt) {
          const u32x2 lo = tr_read(tr + nt * 8), hi = tr_read(tr + nt * 8 + 64);
          wov[2 * kk + nt] = u32x4{lo[0], lo[1], hi[0], hi[1]};
        }
        PIN(wov[2 * kk]); PIN(wov[2 * kk + 1]);
        asm volatile("" ::: "memory");
      }
    }
  });
  {
    const float oa = sw32_sum(acc[0], acc[2]) * fw, ob = sw32_sum(acc[1], acc[3]) * fw;
    *reinterpret_cast<unsigned*>(reinterpret_cast<char*>(ldsu) + recw) = cvtpk(oa, ob);
  }
  BARRIER();
  {
    u32x4 af[4];
#pragma unroll
    for (int kk = 0; kk < 4; ++kk) af[kk] = *reinterpret_cast<const u32x4*>(reinterpret_cast<const char*>(ldsu) + recr + kk * 64);
    asm volatile("" : "+v"(af[0]), "+v"(af[1]), "+v"(af[2]), "+v"(af[3]));
    f32x4 d0 = {0.f, 0.f, 0.f, 0.f}, d1 = {0.f, 0.f, 0.f, 0.f};
#pragma unroll
    for (int kk = 0; kk < 4; ++kk) { d0 = mfma(af[kk], wov[2 * kk], d0); d1 = mfma(af[kk], wov[2 * kk + 1], d1); }
    const float y0 = (d0[0] + d0[1]) + (d0[2] + d0[3]), y1 = (d1[0] + d1[1]) + (d1[2] + d1[3]);
    const float y = x16_sum(sw32_sum(y0, y1)) + fsum;
    const unsigned ya = __float_as_uint(y), yb = __float_as_uint(dpp<XOR1>(y));
    const unsigned w0 = __builtin_amdgcn_perm(yb, ya, 0x03020107u);
    const unsigned w1 = __builtin_amdgcn_perm(yb, tag, 0x06050100u);
    if ((lane & 17) == 0) st8(ws + WS_Y + (size_t)cid * 128, yoff, u32x2{w0, w1});
  }
  if ((b & 1) == 0 && b < 2 * NRED) {
    const int rb = b >> 1;
    const char* ab = ws + WS_Y + (size_t)rb * (NWG * 128);
    const unsigned aoff = (tid >> 6) * 2048 + lane * 16;
    const unsigned t16 = tag & 0xffffu;
    u32x4 va[2];
    for (unsigned spins = 0; spins < SPIN_CAP; ++spins) {
      GLD(va[0], aoff, ab, 0, "sc1"); GLD(va[1], aoff, ab, 1024, "sc1");
      WAITVM(0); PIN(va[0]); PIN(va[1]);
      if (__all((va[0][1] & 0xffffu) == t16 && (va[0][3] & 0xffffu) == t16 && (va[1][1] & 0xffffu) == t16 && (va[1][3] & 0xffffu) == t16)) break;
      __builtin_amdgcn_s_sleep(RSLP);
    }
    float a[4];
#pragma unroll
    for (int k = 0; k < 2; ++k) {
      a[2 * k] = __uint_as_float(va[0][2 * k] & 0xffffff00u) + __uint_as_float(va[1][2 * k] & 0xffffff00u);
      a[2 * k + 1] = __uint_as_float(__builtin_amdgcn_perm(va[0][2 * k], va[0][2 * k + 1], 0x0403020cu))
                   + __uint_as_float(__builtin_amdgcn_perm(va[1][2 * k], va[1][2 * k + 1], 0x0403020cu));
    }
    float s = sw16_sum(sw32_sum(a[0], a[2]), sw32_sum(a[1], a[3]));
    s += dpp<XOR8>(s);
    if (c < 8) lds[L_RA + wave * 32 + c * 4 + r] = s;
    BARRIER();
    if (wave == 0) {
      const int col = lane & 31, hf = lane >> 5;
      const unsigned xv = reinterpret_cast<const unsigned short*>(ldsu + L_X)[rb * 32 + col];
      float sa = 0.f;
#pragma unroll
      for (int w = 0; w < 8; ++w) sa += lds[L_RA + (hf * 8 + w) * 32 + col];
      const float res = bflo(xv) + x32_sum(sa);
      if (lane < 32) *reinterpret_cast<unsigned short*>(out + (rb * 32 + col) * 2) = (unsigned short)(cvtpk(res, res) & 0xffffu);
      if (rb == 0 && lane == 0) __hip_atomic_store(reinterpret_cast<unsigned*>(ws + WS_E), tag, __ATOMIC_RELAXED, __HIP_MEMORY_SCOPE_AGENT);
    }
  }
}

extern "C" void kernel_launch(void* const* d_in, const int* in_sizes, int n_in, void* d_out, int out_size, void* d_ws, size_t ws_size,
                              hipStream_t stream) {
  fused_kernel<<<NWG, NT, 0, stream>>>((const char*)d_in[0], (const char*)d_in[1], (const char*)d_in[2], (const char*)d_in[3], (const char*)d_in[4],
                                       (const char*)d_in[6], (const char*)d_in[7], (const char*)d_in[8], (char*)d_ws, (char*)d_out);
}
```

```cpp
#include <hip/hip_runtime.h>
#include <hip/hip_bf16.h>
#include <stdint.h>
#include <utility>

typedef unsigned u32x4 __attribute__((ext_vector_type(4)));
typedef unsigned u32x2 __attribute__((ext_vector_type(2)));
typedef float f32x4 __attribute__((ext_vector_type(4)));
typedef float f32x2 __attribute__((ext_vector_type(2)));
typedef short s16x4 __attribute__((ext_vector_type(4)));
typedef __bf16 bf16x2 __attribute__((ext_vector_type(2)));
typedef __bf16 bf16x8 __attribute__((ext_vector_type(8)));

constexpr int DM = 512, NH = 32, DH = 128, FF = 1792;
constexpr int NW = 16, NT = NW * 64;
constexpr int NWG = 256;
constexpr int U = 4;
constexpr int CHUNK = 1 << 20;
constexpr int TILEB = NW * U * 1024;
constexpr int NQ = CHUNK / TILEB;
constexpr int NRED = 16;
constexpr unsigned SPIN_CAP = 1u << 18;
constexpr int PH = 3;
constexpr unsigned ROTB = 32768;
constexpr int JWO = 2;
constexpr int JTR = 20;
constexpr int JFF = 10;
constexpr int JLS = 23;
constexpr size_t WS_E = 0, WS_S = 4096, WS_Q = 8192, WS_H = WS_Q + NH * 2048, WS_Y = WS_H + 4 * FF * 8;

__device__ __forceinline__ float dot2(unsigned a, unsigned b, float acc) {
  return __builtin_amdgcn_fdot2_f32_bf16(__builtin_bit_cast(bf16x2, a), __builtin_bit_cast(bf16x2, b), acc, false);
}
template <int CTRL> __device__ __forceinline__ float dpp(float x) {
  return __builtin_bit_cast(float, __builtin_amdgcn_mov_dpp(__builtin_bit_cast(int, x), CTRL, 0xf, 0xf, true));
}
constexpr int XOR1 = 0xB1, XOR2 = 0x4E, XOR7 = 0x141, XOR8 = 0x128, ROR4 = 0x124;
__device__ __forceinline__ float row16_sum(float x) { x += dpp<XOR8>(x); x += dpp<XOR7>(x); x += dpp<XOR2>(x); x += dpp<XOR1>(x); return x; }
__device__ __forceinline__ float row16_max(float x) { x = fmaxf(x, dpp<XOR8>(x)); x = fmaxf(x, dpp<XOR7>(x)); x = fmaxf(x, dpp<XOR2>(x)); x = fmaxf(x, dpp<XOR1>(x)); return x; }
__device__ __forceinline__ float sw16_sum(float a, float b) {
  auto s = __builtin_amdgcn_permlane16_swap(__float_as_uint(a), __float_as_uint(b), false, false);
  return __uint_as_float(s[0]) + __uint_as_float(s[1]);
}
__device__ __forceinline__ float sw32_sum(float a, float b) {
  auto t = __builtin_amdgcn_permlane32_swap(__float_as_uint(a), __float_as_uint(b), false, false);
  return __uint_as_float(t[0]) + __uint_as_float(t[1]);
}
__device__ __forceinline__ float x16_sum(float x) { return sw16_sum(x, x); }
__device__ __forceinline__ float x32_sum(float x) { return sw32_sum(x, x); }
__device__ __forceinline__ float xrow_sum(float x) { return x32_sum(x16_sum(x)); }
__device__ __forceinline__ float xrow_max(float x) {
  auto s = __builtin_amdgcn_permlane16_swap(__float_as_uint(x), __float_as_uint(x), false, false);
  x = fmaxf(__uint_as_float(s[0]), __uint_as_float(s[1]));
  auto t = __builtin_amdgcn_permlane32_swap(__float_as_uint(x), __float_as_uint(x), false, false);
  return fmaxf(__uint_as_float(t[0]), __uint_as_float(t[1]));
}
__device__ __forceinline__ float bflo(unsigned u) { return __uint_as_float(u << 16); }
__device__ __forceinline__ float bfhi(unsigned u) { return __uint_as_float(u & 0xffff0000u); }
__device__ __forceinline__ unsigned cvtpk(float lo, float hi) { bf16x2 r; r[0] = (__bf16)lo; r[1] = (__bf16)hi; return __builtin_bit_cast(unsigned, r); }
__device__ __forceinline__ float rbf(float x) { return (float)(__bf16)x; }
__device__ __forceinline__ f32x4 mfma(u32x4 a, u32x4 b, f32x4 c) {
  return __builtin_amdgcn_mfma_f32_16x16x32_bf16(__builtin_bit_cast(bf16x8, a), __builtin_bit_cast(bf16x8, b), c, 0, 0, 0);
}
__device__ __forceinline__ unsigned pair_lo(unsigned a, unsigned b) { return __builtin_amdgcn_perm(b, a, 0x05040100u); }
__device__ __forceinline__ unsigned pair_hi(unsigned a, unsigned b) { return __builtin_amdgcn_perm(b, a, 0x07060302u); }
__device__ __forceinline__ f32x2 reduce16(const float (&y)[8]) {
  float z[4]; f32x2 t;
#pragma unroll
  for (int j = 0; j < 4; ++j) z[j] = sw32_sum(y[j], y[j + 4]);
#pragma unroll
  for (int j = 0; j < 2; ++j) { float v = sw16_sum(z[j], z[j + 2]); v += dpp<XOR8>(v); v += dpp<ROR4>(v); t[j] = v; }
  return t;
}
__device__ __forceinline__ u32x2 tr_read(const unsigned* p) {
  return __builtin_bit_cast(u32x2, __builtin_amdgcn_ds_read_tr16_b64_v4i16((__attribute__((address_space(3))) s16x4*)p));
}

#define GLD(dst, voff, sbase, OFF, POL) asm volatile("global_load_dwordx4 %0, %1, %2 offset:%3 " POL : "=v"(dst) : "v"(voff), "s"(sbase), "i"(OFF) : "memory")
#define GLD2(dst, voff, sbase, POL) asm volatile("global_load_dwordx2 %0, %1, %2 " POL : "=v"(dst) : "v"(voff), "s"(sbase) : "memory")
#define WAITVM(N) asm volatile("s_waitcnt vmcnt(%0)" :: "i"(N) : "memory")
#define WAITLDS() asm volatile("s_waitcnt lgkmcnt(0)" ::: "memory")
#define PIN(x) asm volatile("" : "+v"(x))
#define BARRIER() do { WAITLDS(); __builtin_amdgcn_s_barrier(); asm volatile("" ::: "memory"); } while (0)
__device__ __forceinline__ void st16(const char* sbase, unsigned voff, u32x4 v) {
  asm volatile("global_store_dwordx4 %0, %1, %2 sc1\n\ts_nop 1" :: "v"(voff), "v"(v), "s"(sbase) : "memory");
}
__device__ __forceinline__ void st8(const char* sbase, unsigned voff, u32x2 v) {
  asm volatile("global_store_dwordx2 %0, %1, %2 sc1\n\ts_nop 1" :: "v"(voff), "v"(v), "s"(sbase) : "memory");
}

constexpr int L_Q = 0;
constexpr int L_TAG = L_Q + 64;
constexpr int L_FP = L_TAG + 4;
constexpr int L_M = L_FP + 256;
constexpr int L_L = L_M + 16;
constexpr int L_REC = L_L + 16;
constexpr int L_RA = L_REC + 16 * 68;
constexpr int L_TR = L_RA + 512;
constexpr int L_X = L_TR + 16 * 512;
constexpr int L_END = L_X + 256;

constexpr bool reissue(int j) { return j + 2 >= PH && j + 2 < 2 * NQ; }
constexpr bool wostep(int j) { return j >= JWO && j < JWO + 8; }
constexpr int younger(int j) {
  int n = 0, pos = -1;
  for (int i = 0; i < PH; ++i) { n += U; if (i == j) pos = n; }
  for (int i = 0; i < j; ++i) {
    if (reissue(i)) { n += U; if (i + 2 == j) pos = n; }
    if (wostep(i)) n += 1;
  }
  return n - pos;
}

template <typename F, int... I> __device__ __forceinline__ void static_for_impl(F&& f, std::integer_sequence<int, I...>) { (f(std::integral_constant<int, I>{}), ...); }
template <int N, typename F> __device__ __forceinline__ void static_for(F&& f) { static_for_impl(f, std::make_integer_sequence<int, N>{}); }

__global__ void __launch_bounds__(NT, 4) fused_kernel(const char* __restrict__ x, const char* __restrict__ kc, const char* __restrict__ vc,
                                                      const char* __restrict__ wq, const char* __restrict__ wo,
                                                      const char* __restrict__ wg, const char* __restrict__ wu, const char* __restrict__ wd,
                                                      char* __restrict__ ws, char* __restrict__ out) {
  __shared__ __attribute__((aligned(16))) unsigned ldsu[L_END];
  float* lds = reinterpret_cast<float*>(ldsu);
  const int tid = threadIdx.x, lane = tid & 63, wave = __builtin_amdgcn_readfirstlane(tid >> 6);
  const int r = lane >> 4, c = lane & 15;
  const int b = blockIdx.x;
  const int xcd = b & 7, idx = b >> 3;
  const int cid = xcd * 32 + idx;
  const int head = cid >> 3, split = cid & 7;
  const unsigned rowsub = lane >> 2, cq = lane & 3;
  unsigned tagw = 0;

  if (wave == 0 || (wave == 1 && idx < 28)) {
    __builtin_amdgcn_s_setprio(3);
    const char* wp; const char* pub; unsigned rstride; int rh;
    if (wave == 0) { rstride = NH * DH * 2; rh = split >> 2; wp = wq + head * (DH * 2) + (split & 3) * 64; pub = ws + WS_Q + head * 2048 + rh * 1024 + (split & 3) * 256; }
    else { const int cg = xcd * 7 + idx % 7, mr = idx / 7;
      rstride = FF * 2; rh = mr & 1; wp = ((mr >> 1) ? wu : wg) + cg * 64; pub = ws + WS_H + mr * (FF * 8) + cg * 256; }
    wp += (size_t)(rh * 256) * rstride;
    const unsigned wvo = rowsub * 16 * rstride + cq * 16;
    u32x4 xf, xa, xb, wv[16];
    unsigned ep;
    GLD(xf, lane * 16, x, 0, "");
    GLD(xa, rowsub * 32, x + rh * 512, 0, ""); GLD(xb, rowsub * 32, x + rh * 512, 16, "");
    asm volatile("global_load_dword %0, %1, %2 sc1" : "=v"(ep) : "v"(0), "s"(ws + WS_E) : "memory");
#pragma unroll
    for (int i = 0; i < 16; ++i) GLD(wv[i], wvo, wp + (size_t)i * rstride, 0, "nt");
    if (wave == 0 && lane == 0) ldsu[L_TAG + 1] = 0u;
    BARRIER();
    WAITVM(16); PIN(xf); PIN(xa); PIN(xb); PIN(ep);
    if (wave == 0) *reinterpret_cast<u32x4*>(ldsu + L_X + lane * 4) = xf;
    unsigned xn[8];
    {
      float e[8];
#pragma unroll
      for (int k = 0; k < 4; ++k) { e[2 * k] = bflo(xf[k]); e[2 * k + 1] = bfhi(xf[k]); }
      float s = ((e[0] + e[1]) + (e[2] + e[3])) + ((e[4] + e[5]) + (e[6] + e[7]));
      s = xrow_sum(row16_sum(s));
      const float mean = s * (1.f / DM);
      float v = 0.f;
#pragma unroll
      for (int k = 0; k < 8; ++k) { const float d = e[k] - mean; v = fmaf(d, d, v); }
      v = xrow_sum(row16_sum(v)) * (1.f / DM);
      const float mean_b = rbf(mean), inv = 1.f / rbf(sqrtf(rbf(rbf(v) + 1e-5f)));
#pragma unroll
      for (int k = 0; k < 4; ++k) {
        xn[k] = cvtpk(rbf(bflo(xa[k]) - mean_b) * inv, rbf(bfhi(xa[k]) - mean_b) * inv);
        xn[4 + k] = cvtpk(rbf(bflo(xb[k]) - mean_b) * inv, rbf(bfhi(xb[k]) - mean_b) * inv);
      }
#pragma unroll
      for (int k = 0; k < 8; ++k) PIN(xn[k]);
    }
    tagw = ep + 1u;
    WAITVM(0);
#pragma unroll
    for (int i = 0; i < 16; ++i) PIN(wv[i]);
    float a[8];
#pragma unroll
    for (int k = 0; k < 8; ++k) a[k] = 0.f;
#pragma unroll
    for (int i = 0; i < 8; ++i)
#pragma unroll
      for (int k = 0; k < 4; ++k) {
        a[2 * k] = dot2(pair_lo(wv[2 * i][k], wv[2 * i + 1][k]), xn[i], a[2 * k]);
        a[2 * k + 1] = dot2(pair_hi(wv[2 * i][k], wv[2 * i + 1][k]), xn[i], a[2 * k + 1]);
      }
    const f32x2 t = reduce16(a);
    if (c < 4) st16(pub, (c * 4 + r) * 16, u32x4{__float_as_uint(t[0]), tagw, __float_as_uint(t[1]), tagw});
    if (wave != 0) __builtin_amdgcn_s_setprio(0);
  } else {
    __builtin_amdgcn_s_barrier();
  }

  const char* kb = kc + (size_t)cid * CHUNK;
  const char* vb = vc + (size_t)cid * CHUNK;
  const unsigned kvo = lane * 16;
  const unsigned rot0 = (unsigned)cid * ROTB + wave * (U * 1024);
  u32x4 KV[2 * NQ][U];
#define ISSUE(j_) do { const char* p_ = ((j_) >= NQ ? vb : kb) + ((((j_) % NQ) * TILEB + rot0) & (CHUNK - 1)); \
    GLD(KV[j_][0], kvo, p_, 0, "nt"); GLD(KV[j_][1], kvo, p_, 1024, "nt"); GLD(KV[j_][2], kvo, p_, 2048, "nt"); GLD(KV[j_][3], kvo, p_, 3072, "nt"); } while (0)
#pragma unroll
  for (int j = 0; j < PH; ++j) ISSUE(j);
  if (wave == 0) {
    const char* qb = ws + WS_Q + head * 2048;
    u32x4 pa[2], pb[2];
    __builtin_amdgcn_s_sleep(8);
    GLD(pa[0], lane * 16, qb, 0, "sc1"); GLD(pa[1], lane * 16, qb, 1024, "sc1");
    __builtin_amdgcn_s_sleep(20);
    GLD(pb[0], lane * 16, qb, 0, "sc1"); GLD(pb[1], lane * 16, qb, 1024, "sc1");
    WAITVM(2); PIN(pa[0]); PIN(pa[1]);
#define QOK(p) __all(p[0][1] == tagw && p[0][3] == tagw && p[1][1] == tagw && p[1][3] == tagw)
    if (!QOK(pa)) {
      WAITVM(0);
#pragma unroll
      for (int j = 0; j < 2; ++j)
        asm volatile("v_mov_b32 %0, %4\n\tv_mov_b32 %1, %5\n\tv_mov_b32 %2, %6\n\tv_mov_b32 %3, %7"
                     : "=&v"(pa[j][0]), "=&v"(pa[j][1]), "=&v"(pa[j][2]), "=&v"(pa[j][3]) : "v"(pb[j][0]), "v"(pb[j][1]), "v"(pb[j][2]), "v"(pb[j][3]));
      for (unsigned spins = 0; !QOK(pa) && spins < SPIN_CAP; ++spins) {
        GLD(pa[0], lane * 16, qb, 0, "sc1"); GLD(pa[1], lane * 16, qb, 1024, "sc1");
        WAITVM(0); PIN(pa[0]); PIN(pa[1]);
      }
    }
    ldsu[L_Q + lane] = cvtpk(__uint_as_float(pa[0][0]) + __uint_as_float(pa[1][0]), __uint_as_float(pa[0][2]) + __uint_as_float(pa[1][2]));
    if (lane == 0) ldsu[L_TAG] = tagw;
    WAITLDS();
    if (lane == 0) __hip_atomic_store(&ldsu[L_TAG + 1], 1u, __ATOMIC_RELAXED, __HIP_MEMORY_SCOPE_WORKGROUP);
    asm volatile("s_waitcnt vmcnt(0)" :: "v"(pb[0]), "v"(pb[1]) : "memory");
    __builtin_amdgcn_s_setprio(0);
  }
  u32x4 mE, mO;
#pragma unroll
  for (int j = 0; j < 4; ++j) {
    const unsigned mj = (((c & 7) >> 1) == j) ? ((c & 1) ? 0xffff0000u : 0x0000ffffu) : 0u;
    mE[j] = c < 8 ? mj : 0u; mO[j] = c < 8 ? 0u : mj;
  }
  while (__hip_atomic_load(&ldsu[L_TAG + 1], __ATOMIC_RELAXED, __HIP_MEMORY_SCOPE_WORKGROUP) == 0u) __builtin_amdgcn_s_sleep(2);
  asm volatile("" ::: "memory");
  const u32x4 qpk = *reinterpret_cast<const u32x4*>(ldsu + L_Q + c * 4);
  const unsigned tag = ldsu[L_TAG];

  constexpr float SCALE = 0.088388347648318440f, LOG2E = 1.4426950408889634f;
  unsigned sp[NQ];
  float mx = -INFINITY, Mw = 0.f, fsum = 0.f, fw = 0.f;
  f32x4 acc = {0.f, 0.f, 0.f, 0.f};
  u32x4 wov[8], wdv, sv;
  u32x2 hv;
  const bool hi8 = c >= 8;
  const int cj = xcd * 2 + (idx & 1), kj = idx >> 1;
  const unsigned colo = rowsub * (DM * 2) + cq * 16;
  const unsigned hvo = cq * (FF * 8) + rowsub * 8;
  unsigned recw, recr, yoff;

  static_for<2 * NQ>([&](auto jc_) __attribute__((always_inline)) {
    constexpr int j = decltype(jc_)::value;
    constexpr int WJ = younger(j);
    if constexpr (j < NQ) {
      float dd[4], t[2];
      WAITVM(WJ);
#pragma unroll
      for (int i = 0; i < 4; ++i) {
        PIN(KV[j][i]);
        dd[i] = dot2(KV[j][i][0], qpk[0], 0.f);
#pragma unroll
        for (int k = 1; k < 4; ++k) dd[i] = dot2(KV[j][i][k], qpk[k], dd[i]);
      }
#pragma unroll
      for (int i = 0; i < 4; ++i) PIN(dd[i]);
      if constexpr (reissue(j)) ISSUE(j + 2);
#pragma unroll
      for (int i = 0; i < 2; ++i) {
        t[i] = hi8 ? dd[2 * i + 1] : dd[2 * i]; const float o = hi8 ? dd[2 * i] : dd[2 * i + 1];
        t[i] += dpp<XOR8>(o); t[i] += dpp<XOR7>(t[i]); t[i] += dpp<XOR2>(t[i]); t[i] += dpp<XOR1>(t[i]);
      }
      const unsigned raw = cvtpk(t[0], t[1]);
      sp[j] = cvtpk(bflo(raw) * SCALE, bfhi(raw) * SCALE);
      mx = fmaxf(mx, fmaxf(bflo(sp[j]), bfhi(sp[j])));
      PIN(sp[j]); PIN(mx);
      if constexpr (j == NQ - 1) {
        mx = xrow_max(fmaxf(mx, dpp<XOR8>(mx)));
        if (lane == 0) lds[L_M + wave] = mx;
      }
    } else {
      if constexpr (j == NQ) {
        Mw = row16_max(lds[L_M + c]);
        float l = 0.f;
#pragma unroll
        for (int i = 0; i < NQ; ++i) {
          const float p0 = __builtin_amdgcn_exp2f((bflo(sp[i]) - Mw) * LOG2E), p1 = __builtin_amdgcn_exp2f((bfhi(sp[i]) - Mw) * LOG2E);
          l += p0 + p1;
          sp[i] = cvtpk(p0, p1);
          PIN(sp[i]);
        }
        l = xrow_sum(l + dpp<XOR8>(l));
        if (lane == 0) lds[L_L + wave] = l;
      }
      const unsigned pp0 = pair_lo(sp[j - NQ], sp[j - NQ]), pp1 = pair_hi(sp[j - NQ], sp[j - NQ]);
      u32x4 a0 = mE & pp0, a1 = mO & pp0, a2 = mE & pp1, a3 = mO & pp1;
      if constexpr (j == 2 * NQ - 1) {
        fw = lds[L_TAG + 2];
        recw = (L_REC + wave * 68 + c * 4 + (r & 1) * 2 + (r >> 1)) * 4; recr = (L_REC + c * 68 + r * 4) * 4;
        yoff = (unsigned)(tid >> 6) * (NWG * 128) + (lane >> 5) * 64 + c * 4;
        PIN(fw); PIN(recw); PIN(recr); PIN(yoff); PIN(a0); PIN(a1); PIN(a2); PIN(a3);
      }
      WAITVM(WJ);
#pragma unroll
      for (int i = 0; i < 4; ++i) PIN(KV[j][i]);
      acc = mfma(a0, KV[j][0], acc); acc = mfma(a1, KV[j][1], acc); acc = mfma(a2, KV[j][2], acc); acc = mfma(a3, KV[j][3], acc);
      PIN(acc);
      if constexpr (reissue(j)) ISSUE(j + 2);
    }
    if constexpr (wostep(j)) GLD(wov[j - JWO], colo + (tid >> 6) * 64, wo + (size_t)(head * DH + (j - JWO) * 16) * (DM * 2), 0, "nt");
    if constexpr (j < 2 * NQ - 2) BARRIER();
    if constexpr (j == JFF) {
      if (wave < 7) {
        GLD(wdv, colo, wd + (size_t)(kj * 112 + wave * 16) * (DM * 2) + cj * 64, 0, "nt");
        GLD2(hv, hvo, ws + WS_H + (kj * 112 + wave * 16) * 8, "sc1");
      }
    }
    if constexpr (j == JFF + 3) {
      if (wave < 7) {
        PIN(wdv); PIN(hv);
        for (unsigned spins = 0; !__all(hv[1] == tag) && spins < SPIN_CAP; ++spins) {
          __builtin_amdgcn_s_sleep(8);
          GLD2(hv, hvo, ws + WS_H + (kj * 112 + wave * 16) * 8, "sc1");
          WAITVM(0); PIN(hv);
        }
        float gu = __uint_as_float(hv[0]); gu += dpp<XOR1>(gu);
        const float gs = rbf(dpp<0x00>(gu)), us = rbf(dpp<0xAA>(gu));
        const float h = rbf(rbf(1.f / (1.f + __expf(-gs))) * us);
        float y[8];
#pragma unroll
        for (int k = 0; k < 4; ++k) { y[2 * k] = h * bflo(wdv[k]); y[2 * k + 1] = h * bfhi(wdv[k]); }
        const f32x2 f2 = reduce16(y);
        if (c < 4) *reinterpret_cast<f32x2*>(lds + L_FP + wave * 32 + c * 8 + r * 2) = f2;
      }
    }
    if constexpr (j == JFF + 4) {
      if (wave == cj) {
#pragma unroll
        for (int w = 0; w < 7; ++w) fsum += lds[L_FP + w * 32 + (lane >> 5) * 16 + c];
      }
      PIN(fsum);
    }
    if constexpr (j == NQ) {
      if (wave == 0) {
        const float L = row16_sum(lds[L_L + c]);
        if (lane == 0) st16(ws + WS_S + cid * 16, 0, u32x4{__float_as_uint(Mw), tag, __float_as_uint(L), tag});
      }
    }
    if constexpr (j == JLS) {
      if (wave == 0) GLD(sv, (lane & 7) * 16, ws + WS_S + head * 128, 0, "sc1");
    }
    if constexpr (j == JLS + 3) {
      if (wave == 0) {
        PIN(sv);
        for (unsigned spins = 0; !__all(sv[1] == tag && sv[3] == tag) && spins < SPIN_CAP; ++spins) {
          __builtin_amdgcn_s_sleep(8);
          GLD(sv, (lane & 7) * 16, ws + WS_S + head * 128, 0, "sc1");
          WAITVM(0); PIN(sv);
        }
        const float ms = __uint_as_float(sv[0]);
        float S = fmaxf(ms, dpp<XOR1>(ms)); S = fmaxf(S, dpp<XOR2>(S)); S = fmaxf(S, dpp<XOR7>(S));
        float e = __builtin_amdgcn_exp2f((ms - S) * LOG2E) * __uint_as_float(sv[2]);
        e += dpp<XOR1>(e); e += dpp<XOR2>(e); e += dpp<XOR7>(e);
        if (lane == 0) lds[L_TAG + 2] = __builtin_amdgcn_exp2f((Mw - S) * LOG2E) / e;
      }
    }
    if constexpr (j == JTR) {
      unsigned* tb = ldsu + L_TR + wave * 512;
      const unsigned* tr = tb + (r * 8 + ((lane >> 2) & 3)) * 16 + (lane & 3) * 2;
#pragma unroll
      for (int kk = 0; kk < 4; ++kk) {
        PIN(wov[2 * kk]); PIN(wov[2 * kk + 1]);
        *reinterpret_cast<u32x4*>(tb + lane * 4) = wov[2 * kk];
        *reinterpret_cast<u32x4*>(tb + 256 + lane * 4) = wov[2 * kk + 1];
        asm volatile("" ::: "memory");
#pragma unroll
        for (int nt = 0; nt < 2; ++nt) {
          const u32x2 lo = tr_read(tr + nt * 8), hi = tr_read(tr + nt * 8 + 64);
          wov[2 * kk + nt] = u32x4{lo[0], lo[1], hi[0], hi[1]};
        }
        PIN(wov[2 * kk]); PIN(wov[2 * kk + 1]);
        asm volatile("" ::: "memory");
      }
    }
  });
  {
    const float oa = sw32_sum(acc[0], acc[2]) * fw, ob = sw32_sum(acc[1], acc[3]) * fw;
    *reinterpret_cast<unsigned*>(reinterpret_cast<char*>(ldsu) + recw) = cvtpk(oa, ob);
  }
  BARRIER();
  {
    u32x4 af[4];
#pragma unroll
    for (int kk = 0; kk < 4; ++kk) af[kk] = *reinterpret_cast<const u32x4*>(reinterpret_cast<const char*>(ldsu) + recr + kk * 64);
    asm volatile("" : "+v"(af[0]), "+v"(af[1]), "+v"(af[2]), "+v"(af[3]));
    f32x4 d0 = {0.f, 0.f, 0.f, 0.f}, d1 = {0.f, 0.f, 0.f, 0.f};
#pragma unroll
    for (int kk = 0; kk < 4; ++kk) { d0 = mfma(af[kk], wov[2 * kk], d0); d1 = mfma(af[kk], wov[2 * kk + 1], d1); }
    const float y0 = (d0[0] + d0[1]) + (d0[2] + d0[3]), y1 = (d1[0] + d1[1]) + (d1[2] + d1[3]);
    const float y = x16_sum(sw32_sum(y0, y1)) + fsum;
    const unsigned ya = __float_as_uint(y), yb = __float_as_uint(dpp<XOR1>(y));
    const unsigned w0 = __builtin_amdgcn_perm(yb, ya, 0x03020107u);
    const unsigned w1 = __builtin_amdgcn_perm(yb, tag, 0x06050100u);
    if ((lane & 17) == 0) st8(ws + WS_Y + (size_t)cid * 128, yoff, u32x2{w0, w1});
  }
  if ((b & 1) == 0 && b < 2 * NRED) {
    const int rb = b >> 1;
    const char* ab = ws + WS_Y + (size_t)rb * (NWG * 128);
    const unsigned aoff = (tid >> 6) * 2048 + lane * 16;
    const unsigned t16 = tag & 0xffffu;
    u32x4 va[2];
    for (unsigned spins = 0; spins < SPIN_CAP; ++spins) {
      GLD(va[0], aoff, ab, 0, "sc1"); GLD(va[1], aoff, ab, 1024, "sc1");
      WAITVM(0); PIN(va[0]); PIN(va[1]);
      if (__all((va[0][1] & 0xffffu) == t16 && (va[0][3] & 0xffffu) == t16 && (va[1][1] & 0xffffu) == t16 && (va[1][3] & 0xffffu) == t16)) break;
    }
    float a[4];
#pragma unroll
    for (int k = 0; k < 2; ++k) {
      a[2 * k] = __uint_as_float(va[0][2 * k] & 0xffffff00u) + __uint_as_float(va[1][2 * k] & 0xffffff00u);
      a[2 * k + 1] = __uint_as_float(__builtin_amdgcn_perm(va[0][2 * k], va[0][2 * k + 1], 0x0403020cu))
                   + __uint_as_float(__builtin_amdgcn_perm(va[1][2 * k], va[1][2 * k + 1], 0x0403020cu));
    }
    float s = sw16_sum(sw32_sum(a[0], a[2]), sw32_sum(a[1], a[3]));
    s += dpp<XOR8>(s);
    if (c < 8) lds[L_RA + wave * 32 + c * 4 + r] = s;
    BARRIER();
    if (wave == 0) {
      const int col = lane & 31, hf = lane >> 5;
      const unsigned xv = reinterpret_cast<const unsigned short*>(ldsu + L_X)[rb * 32 + col];
      float sa = 0.f;
#pragma unroll
      for (int w = 0; w < 8; ++w) sa += lds[L_RA + (hf * 8 + w) * 32 + col];
      const float res = bflo(xv) + x32_sum(sa);
      if (lane < 32) *reinterpret_cast<unsigned short*>(out + (rb * 32 + col) * 2) = (unsigned short)(cvtpk(res, res) & 0xffffu);
      if (rb == 0 && lane == 0) __hip_atomic_store(reinterpret_cast<unsigned*>(ws + WS_E), tag, __ATOMIC_RELAXED, __HIP_MEMORY_SCOPE_AGENT);
    }
  }
}

extern "C" void kernel_launch(void* const* d_in, const int* in_sizes, int n_in, void* d_out, int out_size, void* d_ws, size_t ws_size,
                              hipStream_t stream) {
  fused_kernel<<<NWG, NT, 0, stream>>>((const char*)d_in[0], (const char*)d_in[1], (const char*)d_in[2], (const char*)d_in[3], (const char*)d_in[4],
                                       (const char*)d_in[6], (const char*)d_in[7], (const char*)d_in[8], (char*)d_ws, (char*)d_out);
}
```

```cpp
#include <hip/hip_runtime.h>
#include <hip/hip_bf16.h>
#include <stdint.h>
#include <utility>

typedef unsigned u32x4 __attribute__((ext_vector_type(4)));
typedef unsigned u32x2 __attribute__((ext_vector_type(2)));
typedef float f32x4 __attribute__((ext_vector_type(4)));
typedef float f32x2 __attribute__((ext_vector_type(2)));
typedef short s16x4 __attribute__((ext_vector_type(4)));
typedef __bf16 bf16x2 __attribute__((ext_vector_type(2)));
typedef __bf16 bf16x8 __attribute__((ext_vector_type(8)));

constexpr int DM = 512, NH = 32, DH = 128, FF = 1792;
constexpr int NW = 16, NT = NW * 64;
constexpr int NWG = 256;
constexpr int U = 4;
constexpr int CHUNK = 1 << 20;
constexpr int TILEB = NW * U * 1024;
constexpr int NQ = CHUNK / TILEB;
constexpr int NRED = 16;
constexpr unsigned SPIN_CAP = 1u << 18;
constexpr int PH = 3;
constexpr int LGG = 1;
constexpr unsigned ROTB = 131072;
constexpr unsigned ROTX = 11 * 131072;
constexpr unsigned PUBM = 0x05550000u;
constexpr int JWO = 4;
constexpr int JTR = 20;
constexpr int JFF = 6;
constexpr int JLS = 23;
constexpr size_t WS_E = 0, WS_S = 4096, WS_Q = 8192, WS_H = WS_Q + NH * 2048, WS_Y = WS_H + 4 * FF * 8, WS_P = WS_Y + (size_t)NRED * NWG * 128;

__device__ __forceinline__ float dot2(unsigned a, unsigned b, float acc) {
  return __builtin_amdgcn_fdot2_f32_bf16(__builtin_bit_cast(bf16x2, a), __builtin_bit_cast(bf16x2, b), acc, false);
}
template <int CTRL> __device__ __forceinline__ float dpp(float x) {
  return __builtin_bit_cast(float, __builtin_amdgcn_mov_dpp(__builtin_bit_cast(int, x), CTRL, 0xf, 0xf, true));
}
constexpr int XOR1 = 0xB1, XOR2 = 0x4E, XOR7 = 0x141, XOR8 = 0x128, ROR4 = 0x124;
__device__ __forceinline__ float row16_sum(float x) { x += dpp<XOR8>(x); x += dpp<XOR7>(x); x += dpp<XOR2>(x); x += dpp<XOR1>(x); return x; }
__device__ __forceinline__ float row16_max(float x) { x = fmaxf(x, dpp<XOR8>(x)); x = fmaxf(x, dpp<XOR7>(x)); x = fmaxf(x, dpp<XOR2>(x)); x = fmaxf(x, dpp<XOR1>(x)); return x; }
__device__ __forceinline__ float sw16_sum(float a, float b) {
  auto s = __builtin_amdgcn_permlane16_swap(__float_as_uint(a), __float_as_uint(b), false, false);
  return __uint_as_float(s[0]) + __uint_as_float(s[1]);
}
__device__ __forceinline__ float sw32_sum(float a, float b) {
  auto t = __builtin_amdgcn_permlane32_swap(__float_as_uint(a), __float_as_uint(b), false, false);
  return __uint_as_float(t[0]) + __uint_as_float(t[1]);
}
__device__ __forceinline__ float x16_sum(float x) { return sw16_sum(x, x); }
__device__ __forceinline__ float x32_sum(float x) { return sw32_sum(x, x); }
__device__ __forceinline__ float xrow_sum(float x) { return x32_sum(x16_sum(x)); }
__device__ __forceinline__ float xrow_max(float x) {
  auto s = __builtin_amdgcn_permlane16_swap(__float_as_uint(x), __float_as_uint(x), false, false);
  x = fmaxf(__uint_as_float(s[0]), __uint_as_float(s[1]));
  auto t = __builtin_amdgcn_permlane32_swap(__float_as_uint(x), __float_as_uint(x), false, false);
  return fmaxf(__uint_as_float(t[0]), __uint_as_float(t[1]));
}
__device__ __forceinline__ float bflo(unsigned u) { return __uint_as_float(u << 16); }
__device__ __forceinline__ float bfhi(unsigned u) { return __uint_as_float(u & 0xffff0000u); }
__device__ __forceinline__ unsigned cvtpk(float lo, float hi) { bf16x2 r; r[0] = (__bf16)lo; r[1] = (__bf16)hi; return __builtin_bit_cast(unsigned, r); }
__device__ __forceinline__ float rbf(float x) { return (float)(__bf16)x; }
__device__ __forceinline__ f32x4 mfma(u32x4 a, u32x4 b, f32x4 c) {
  return __builtin_amdgcn_mfma_f32_16x16x32_bf16(__builtin_bit_cast(bf16x8, a), __builtin_bit_cast(bf16x8, b), c, 0, 0, 0);
}
__device__ __forceinline__ unsigned pair_lo(unsigned a, unsigned b) { return __builtin_amdgcn_perm(b, a, 0x05040100u); }
__device__ __forceinline__ unsigned pair_hi(unsigned a, unsigned b) { return __builtin_amdgcn_perm(b, a, 0x07060302u); }
__device__ __forceinline__ f32x2 reduce16(const float (&y)[8]) {
  float z[4]; f32x2 t;
#pragma unroll
  for (int j = 0; j < 4; ++j) z[j] = sw32_sum(y[j], y[j + 4]);
#pragma unroll
  for (int j = 0; j < 2; ++j) { float v = sw16_sum(z[j], z[j + 2]); v += dpp<XOR8>(v); v += dpp<ROR4>(v); t[j] = v; }
  return t;
}
__device__ __forceinline__ u32x2 tr_read(const unsigned* p) {
  return __builtin_bit_cast(u32x2, __builtin_amdgcn_ds_read_tr16_b64_v4i16((__attribute__((address_space(3))) s16x4*)p));
}

#define GLD(dst, voff, sbase, OFF, POL) asm volatile("global_load_dwordx4 %0, %1, %2 offset:%3 " POL : "=v"(dst) : "v"(voff), "s"(sbase), "i"(OFF) : "memory")
#define GLD2(dst, voff, sbase, POL) asm volatile("global_load_dwordx2 %0, %1, %2 " POL : "=v"(dst) : "v"(voff), "s"(sbase) : "memory")
#define WAITVM(N) asm volatile("s_waitcnt vmcnt(%0)" :: "i"(N) : "memory")
#define WAITLDS() asm volatile("s_waitcnt lgkmcnt(0)" ::: "memory")
#define PIN(x) asm volatile("" : "+v"(x))
#define BARRIER() do { WAITLDS(); __builtin_amdgcn_s_barrier(); asm volatile("" ::: "memory"); } while (0)
__device__ __forceinline__ void st16(const char* sbase, unsigned voff, u32x4 v) {
  asm volatile("global_store_dwordx4 %0, %1, %2 sc1\n\ts_nop 1" :: "v"(voff), "v"(v), "s"(sbase) : "memory");
}
__device__ __forceinline__ void st8(const char* sbase, unsigned voff, u32x2 v) {
  asm volatile("global_store_dwordx2 %0, %1, %2 sc1\n\ts_nop 1" :: "v"(voff), "v"(v), "s"(sbase) : "memory");
}

constexpr int L_Q = 0;
constexpr int L_TAG = L_Q + 64;
constexpr int L_FP = L_TAG + 4;
constexpr int L_M = L_FP + 256;
constexpr int L_L = L_M + 16;
constexpr int L_REC = L_L + 16;
constexpr int L_RA = L_REC + 16 * 68;
constexpr int L_TR = L_RA + 512;
constexpr int L_X = L_TR + 16 * 512;
constexpr int L_END = L_X + 256;

constexpr bool reissue(int j) { return j + 2 >= PH && j + 2 < 2 * NQ; }
constexpr bool wostep(int j) { return j >= JWO && j < JWO + 8; }
constexpr int younger(int j) {
  int n = 0, pos = -1;
  for (int i = 0; i < PH; ++i) { n += U; if (i == j) pos = n; }
  for (int i = 0; i < j; ++i) {
    if (reissue(i)) { n += U; if (i + 2 == j) pos = n; }
    if (wostep(i)) n += 1;
  }
  return n - pos;
}

template <typename F, int... I> __device__ __forceinline__ void static_for_impl(F&& f, std::integer_sequence<int, I...>) { (f(std::integral_constant<int, I>{}), ...); }
template <int N, typename F> __device__ __forceinline__ void static_for(F&& f) { static_for_impl(f, std::make_integer_sequence<int, N>{}); }

__global__ void __launch_bounds__(NT, 4) fused_kernel(const char* __restrict__ x, const char* __restrict__ kc, const char* __restrict__ vc,
                                                      const char* __restrict__ wq, const char* __restrict__ wo,
                                                      const char* __restrict__ wg, const char* __restrict__ wu, const char* __restrict__ wd,
                                                      char* __restrict__ ws, char* __restrict__ out) {
  __shared__ __attribute__((aligned(16))) unsigned ldsu[L_END];
  float* lds = reinterpret_cast<float*>(ldsu);
  const int tid = threadIdx.x, lane = tid & 63, wave = __builtin_amdgcn_readfirstlane(tid >> 6);
  const int r = lane >> 4, c = lane & 15;
  const int b = blockIdx.x;
  const int xcd = b & 7, idx = b >> 3;
  const int cid = xcd * 32 + idx;
  const int head = cid >> 3, split = cid & 7;
  const unsigned rowsub = lane >> 2, cq = lane & 3;
  unsigned tagw = 0;

  const bool special = wave == 0 || (wave == 1 && idx < 28);
  if (special) {
    __builtin_amdgcn_s_setprio(3);
    const char* wp; const char* pub; unsigned rstride; int rh;
    if (wave == 0) { rstride = NH * DH * 2; rh = split >> 2; wp = wq + head * (DH * 2) + (split & 3) * 64; pub = ws + WS_Q + head * 2048 + rh * 1024 + (split & 3) * 256; }
    else { const int cg = xcd * 7 + idx % 7, mr = idx / 7;
      rstride = FF * 2; rh = mr & 1; wp = ((mr >> 1) ? wu : wg) + cg * 64; pub = ws + WS_H + mr * (FF * 8) + cg * 256; }
    wp += (size_t)(rh * 256) * rstride;
    const unsigned wvo = rowsub * 16 * rstride + cq * 16;
    u32x4 xf, xa, xb, wv[16];
    unsigned ep;
    GLD(xf, lane * 16, x, 0, "");
    GLD(xa, rowsub * 32, x + rh * 512, 0, ""); GLD(xb, rowsub * 32, x + rh * 512, 16, "");
    asm volatile("global_load_dword %0, %1, %2 sc1" : "=v"(ep) : "v"(0), "s"(ws + WS_E) : "memory");
#pragma unroll
    for (int i = 0; i < 16; ++i) GLD(wv[i], wvo, wp + (size_t)i * rstride, 0, "nt");
    if (wave == 0 && lane == 0) ldsu[L_TAG + 1] = 0u;
    BARRIER();
    WAITVM(16); PIN(xf); PIN(xa); PIN(xb); PIN(ep);
    if (wave == 0) *reinterpret_cast<u32x4*>(ldsu + L_X + lane * 4) = xf;
    unsigned xn[8];
    {
      float e[8];
#pragma unroll
      for (int k = 0; k < 4; ++k) { e[2 * k] = bflo(xf[k]); e[2 * k + 1] = bfhi(xf[k]); }
      float s = ((e[0] + e[1]) + (e[2] + e[3])) + ((e[4] + e[5]) + (e[6] + e[7]));
      s = xrow_sum(row16_sum(s));
      const float mean = s * (1.f / DM);
      float v = 0.f;
#pragma unroll
      for (int k = 0; k < 8; ++k) { const float d = e[k] - mean; v = fmaf(d, d, v); }
      v = xrow_sum(row16_sum(v)) * (1.f / DM);
      const float mean_b = rbf(mean), inv = 1.f / rbf(sqrtf(rbf(rbf(v) + 1e-5f)));
#pragma unroll
      for (int k = 0; k < 4; ++k) {
        xn[k] = cvtpk(rbf(bflo(xa[k]) - mean_b) * inv, rbf(bfhi(xa[k]) - mean_b) * inv);
        xn[4 + k] = cvtpk(rbf(bflo(xb[k]) - mean_b) * inv, rbf(bfhi(xb[k]) - mean_b) * inv);
      }
#pragma unroll
      for (int k = 0; k < 8; ++k) PIN(xn[k]);
    }
    tagw = ep + 1u;
    WAITVM(0);
#pragma unroll
    for (int i = 0; i < 16; ++i) PIN(wv[i]);
    float a[8];
#pragma unroll
    for (int k = 0; k < 8; ++k) a[k] = 0.f;
#pragma unroll
    for (int i = 0; i < 8; ++i)
#pragma unroll
      for (int k = 0; k < 4; ++k) {
        a[2 * k] = dot2(pair_lo(wv[2 * i][k], wv[2 * i + 1][k]), xn[i], a[2 * k]);
        a[2 * k + 1] = dot2(pair_hi(wv[2 * i][k], wv[2 * i + 1][k]), xn[i], a[2 * k + 1]);
      }
    const f32x2 t = reduce16(a);
    if (c < 4) st16(pub, (c * 4 + r) * 16, u32x4{__float_as_uint(t[0]), tagw, __float_as_uint(t[1]), tagw});
    if (wave != 0) __builtin_amdgcn_s_setprio(0);
  }

  const char* kb = kc + (size_t)(cid >> LGG) * (CHUNK << LGG);
  const char* vb = vc + (size_t)(cid >> LGG) * (CHUNK << LGG);
  const unsigned kvo = lane * 16;
  const unsigned rot0 = (unsigned)(cid >> LGG) * ROTB + (cid & ((1 << LGG) - 1)) * TILEB + wave * (U * 1024) + xcd * ROTX;
  u32x4 KV[2 * NQ][U];
#define ISSUE(j_) do { const char* p_ = ((j_) >= NQ ? vb : kb) + ((((j_) % NQ) * (TILEB << LGG) + rot0) & ((CHUNK << LGG) - 1)); \
    GLD(KV[j_][0], kvo, p_, 0, "nt"); GLD(KV[j_][1], kvo, p_, 1024, "nt"); GLD(KV[j_][2], kvo, p_, 2048, "nt"); GLD(KV[j_][3], kvo, p_, 3072, "nt"); } while (0)
  if (!special) __builtin_amdgcn_s_barrier();
#pragma unroll
  for (int j = 0; j < PH; ++j) ISSUE(j);
  if (wave == 0) {
    const char* qb = ws + WS_Q + head * 2048;
    u32x4 pa[2], pb[2];
    __builtin_amdgcn_s_sleep(8);
    GLD(pa[0], lane * 16, qb, 0, "sc1"); GLD(pa[1], lane * 16, qb, 1024, "sc1");
    __builtin_amdgcn_s_sleep(20);
    GLD(pb[0], lane * 16, qb, 0, "sc1"); GLD(pb[1], lane * 16, qb, 1024, "sc1");
    WAITVM(2); PIN(pa[0]); PIN(pa[1]);
#define QOK(p) __all(p[0][1] == tagw && p[0][3] == tagw && p[1][1] == tagw && p[1][3] == tagw)
    if (!QOK(pa)) {
      WAITVM(0);
#pragma unroll
      for (int j = 0; j < 2; ++j)
        asm volatile("v_mov_b32 %0, %4\n\tv_mov_b32 %1, %5\n\tv_mov_b32 %2, %6\n\tv_mov_b32 %3, %7"
                     : "=&v"(pa[j][0]), "=&v"(pa[j][1]), "=&v"(pa[j][2]), "=&v"(pa[j][3]) : "v"(pb[j][0]), "v"(pb[j][1]), "v"(pb[j][2]), "v"(pb[j][3]));
      for (unsigned spins = 0; !QOK(pa) && spins < SPIN_CAP; ++spins) {
        GLD(pa[0], lane * 16, qb, 0, "sc1"); GLD(pa[1], lane * 16, qb, 1024, "sc1");
        WAITVM(0); PIN(pa[0]); PIN(pa[1]);
      }
    }
    ldsu[L_Q + lane] = cvtpk(__uint_as_float(pa[0][0]) + __uint_as_float(pa[1][0]), __uint_as_float(pa[0][2]) + __uint_as_float(pa[1][2]));
    if (lane == 0) ldsu[L_TAG] = tagw;
    WAITLDS();
    if (lane == 0) __hip_atomic_store(&ldsu[L_TAG + 1], 1u, __ATOMIC_RELAXED, __HIP_MEMORY_SCOPE_WORKGROUP);
    asm volatile("s_waitcnt vmcnt(0)" :: "v"(pb[0]), "v"(pb[1]) : "memory");
    __builtin_amdgcn_s_setprio(0);
  }
  u32x4 mE, mO;
#pragma unroll
  for (int j = 0; j < 4; ++j) {
    const unsigned mj = (((c & 7) >> 1) == j) ? ((c & 1) ? 0xffff0000u : 0x0000ffffu) : 0u;
    mE[j] = c < 8 ? mj : 0u; mO[j] = c < 8 ? 0u : mj;
  }
  while (__hip_atomic_load(&ldsu[L_TAG + 1], __ATOMIC_RELAXED, __HIP_MEMORY_SCOPE_WORKGROUP) == 0u) __builtin_amdgcn_s_sleep(2);
  asm volatile("" ::: "memory");
  const u32x4 qpk = *reinterpret_cast<const u32x4*>(ldsu + L_Q + c * 4);
  const unsigned tag = ldsu[L_TAG];

  constexpr float SCALE = 0.088388347648318440f, LOG2E = 1.4426950408889634f;
  unsigned sp[NQ];
  float mx = -INFINITY, Mw = 0.f, fsum = 0.f, fw = 0.f;
  f32x4 acc = {0.f, 0.f, 0.f, 0.f};
  u32x4 wov[8], wdv, sv;
  u32x2 hv;
  const bool hi8 = c >= 8;
  const int cj = xcd * 2 + (idx & 1), kj = idx >> 1;
  const unsigned colo = rowsub * (DM * 2) + cq * 16;
  const unsigned hvo = cq * (FF * 8) + rowsub * 8;
  unsigned recw, recr, yoff;

  u32x2 gv;
  const char* gb = ws + WS_P + (idx >> 1) * 256;
#define ISCK(c_) ((c_) >= 0 && ((PUBM >> ((c_) >= 0 ? (c_) : 0)) & 1u))
#define GATE(j_) do { \
    if constexpr (ISCK((j_) - 2)) { if (wave == NW - 1) { PIN(gv); \
        for (unsigned spins = 0; !__all(gv[1] == tag) && spins < SPIN_CAP; ++spins) { \
          GLD2(gv, c * 8, gb + ((j_) - 2) * 4096, "sc1"); WAITVM(0); PIN(gv); __builtin_amdgcn_s_sleep(12); } } } \
    if constexpr (ISCK((j_) - 1)) { if (wave == NW - 1) GLD2(gv, c * 8, gb + ((j_) - 1) * 4096, "sc1"); } } while (0)

  static_for<2 * NQ>([&](auto jc_) __attribute__((always_inline)) {
    constexpr int j = decltype(jc_)::value;
    constexpr int WJ = younger(j);
    if constexpr (j < NQ) {
      float dd[4], t[2];
      WAITVM(WJ);
#pragma unroll
      for (int i = 0; i < 4; ++i) {
        PIN(KV[j][i]);
        dd[i] = dot2(KV[j][i][0], qpk[0], 0.f);
#pragma unroll
        for (int k = 1; k < 4; ++k) dd[i] = dot2(KV[j][i][k], qpk[k], dd[i]);
      }
#pragma unroll
      for (int i = 0; i < 4; ++i) PIN(dd[i]);
      if constexpr (reissue(j)) ISSUE(j + 2);
#pragma unroll
      for (int i = 0; i < 2; ++i) {
        t[i] = hi8 ? dd[2 * i + 1] : dd[2 * i]; const float o = hi8 ? dd[2 * i] : dd[2 * i + 1];
        t[i] += dpp<XOR8>(o); t[i] += dpp<XOR7>(t[i]); t[i] += dpp<XOR2>(t[i]); t[i] += dpp<XOR1>(t[i]);
      }
      const unsigned raw = cvtpk(t[0], t[1]);
      sp[j] = cvtpk(bflo(raw) * SCALE, bfhi(raw) * SCALE);
      mx = fmaxf(mx, fmaxf(bflo(sp[j]), bfhi(sp[j])));
      PIN(sp[j]); PIN(mx);
      if constexpr (j == NQ - 1) {
        mx = xrow_max(fmaxf(mx, dpp<XOR8>(mx)));
        if (lane == 0) lds[L_M + wave] = mx;
      }
    } else {
      if constexpr (j == NQ) {
        Mw = row16_max(lds[L_M + c]);
        float l = 0.f;
#pragma unroll
        for (int i = 0; i < NQ; ++i) {
          const float p0 = __builtin_amdgcn_exp2f((bflo(sp[i]) - Mw) * LOG2E), p1 = __builtin_amdgcn_exp2f((bfhi(sp[i]) - Mw) * LOG2E);
          l += p0 + p1;
          sp[i] = cvtpk(p0, p1);
          PIN(sp[i]);
        }
        l = xrow_sum(l + dpp<XOR8>(l));
        if (lane == 0) lds[L_L + wave] = l;
      }
      const unsigned pp0 = pair_lo(sp[j - NQ], sp[j - NQ]), pp1 = pair_hi(sp[j - NQ], sp[j - NQ]);
      u32x4 a0 = mE & pp0, a1 = mO & pp0, a2 = mE & pp1, a3 = mO & pp1;
      if constexpr (j == 2 * NQ - 1) {
        fw = lds[L_TAG + 2];
        recw = (L_REC + wave * 68 + c * 4 + (r & 1) * 2 + (r >> 1)) * 4; recr = (L_REC + c * 68 + r * 4) * 4;
        yoff = (unsigned)(tid >> 6) * (NWG * 128) + (lane >> 5) * 64 + c * 4;
        PIN(fw); PIN(recw); PIN(recr); PIN(yoff); PIN(a0); PIN(a1); PIN(a2); PIN(a3);
      }
      WAITVM(WJ);
#pragma unroll
      for (int i = 0; i < 4; ++i) PIN(KV[j][i]);
      acc = mfma(a0, KV[j][0], acc); acc = mfma(a1, KV[j][1], acc); acc = mfma(a2, KV[j][2], acc); acc = mfma(a3, KV[j][3], acc);
      PIN(acc);
      GATE(j);
      if constexpr (reissue(j)) ISSUE(j + 2);
    }
    if constexpr (wostep(j)) GLD(wov[j - JWO], colo + (tid >> 6) * 64, wo + (size_t)(head * DH + (j - JWO) * 16) * (DM * 2), 0, "nt");
    if constexpr (j < 2 * NQ - 2) BARRIER();
    if constexpr (ISCK(j)) {
      if (wave == 1 && lane == 0) st8(gb + j * 4096 + ((idx & 1) * 8 + xcd) * 8, 0, u32x2{(unsigned)j, tag});
    }
    if constexpr (j == JFF) {
      if (wave < 7) {
        GLD(wdv, colo, wd + (size_t)(kj * 112 + wave * 16) * (DM * 2) + cj * 64, 0, "nt");
        GLD2(hv, hvo, ws + WS_H + (kj * 112 + wave * 16) * 8, "sc1");
      }
    }
    if constexpr (j == JFF + 3) {
      if (wave < 7) {
        PIN(wdv); PIN(hv);
        for (unsigned spins = 0; !__all(hv[1] == tag) && spins < SPIN_CAP; ++spins) {
          __builtin_amdgcn_s_sleep(8);
          GLD2(hv, hvo, ws + WS_H + (kj * 112 + wave * 16) * 8, "sc1");
          WAITVM(0); PIN(hv);
        }
        float gu = __uint_as_float(hv[0]); gu += dpp<XOR1>(gu);
        const float gs = rbf(dpp<0x00>(gu)), us = rbf(dpp<0xAA>(gu));
        const float h = rbf(rbf(1.f / (1.f + __expf(-gs))) * us);
        float y[8];
#pragma unroll
        for (int k = 0; k < 4; ++k) { y[2 * k] = h * bflo(wdv[k]); y[2 * k + 1] = h * bfhi(wdv[k]); }
        const f32x2 f2 = reduce16(y);
        if (c < 4) *reinterpret_cast<f32x2*>(lds + L_FP + wave * 32 + c * 8 + r * 2) = f2;
      }
    }
    if constexpr (j == JFF + 4) {
      if (wave == cj) {
#pragma unroll
        for (int w = 0; w < 7; ++w) fsum += lds[L_FP + w * 32 + (lane >> 5) * 16 + c];
      }
      PIN(fsum);
    }
    if constexpr (j == NQ) {
      if (wave == 0) {
        const float L = row16_sum(lds[L_L + c]);
        if (lane == 0) st16(ws + WS_S + cid * 16, 0, u32x4{__float_as_uint(Mw), tag, __float_as_uint(L), tag});
      }
    }
    if constexpr (j == JLS) {
      if (wave == 0) GLD(sv, (lane & 7) * 16, ws + WS_S + head * 128, 0, "sc1");
    }
    if constexpr (j == JLS + 3) {
      if (wave == 0) {
        PIN(sv);
        for (unsigned spins = 0; !__all(sv[1] == tag && sv[3] == tag) && spins < SPIN_CAP; ++spins) {
          __builtin_amdgcn_s_sleep(8);
          GLD(sv, (lane & 7) * 16, ws + WS_S + head * 128, 0, "sc1");
          WAITVM(0); PIN(sv);
        }
        const float ms = __uint_as_float(sv[0]);
        float S = fmaxf(ms, dpp<XOR1>(ms)); S = fmaxf(S, dpp<XOR2>(S)); S = fmaxf(S, dpp<XOR7>(S));
        float e = __builtin_amdgcn_exp2f((ms - S) * LOG2E) * __uint_as_float(sv[2]);
        e += dpp<XOR1>(e); e += dpp<XOR2>(e); e += dpp<XOR7>(e);
        if (lane == 0) lds[L_TAG + 2] = __builtin_amdgcn_exp2f((Mw - S) * LOG2E) / e;
      }
    }
    if constexpr (j == JTR) {
      unsigned* tb = ldsu + L_TR + wave * 512;
      const unsigned* tr = tb + (r * 8 + ((lane >> 2) & 3)) * 16 + (lane & 3) * 2;
#pragma unroll
      for (int kk = 0; kk < 4; ++kk) {
        PIN(wov[2 * kk]); PIN(wov[2 * kk + 1]);
        *reinterpret_cast<u32x4*>(tb + lane * 4) = wov[2 * kk];
        *reinterpret_cast<u32x4*>(tb + 256 + lane * 4) = wov[2 * kk + 1];
        asm volatile("" ::: "memory");
#pragma unroll
        for (int nt = 0; nt < 2; ++nt) {
          const u32x2 lo = tr_read(tr + nt * 8), hi = tr_read(tr + nt * 8 + 64);
          wov[2 * kk + nt] = u32x4{lo[0], lo[1], hi[0], hi[1]};
        }
        PIN(wov[2 * kk]); PIN(wov[2 * kk + 1]);
        asm volatile("" ::: "memory");
      }
    }
  });
  {
    const float oa = sw32_sum(acc[0], acc[2]) * fw, ob = sw32_sum(acc[1], acc[3]) * fw;
    *reinterpret_cast<unsigned*>(reinterpret_cast<char*>(ldsu) + recw) = cvtpk(oa, ob);
  }
  BARRIER();
  {
    u32x4 af[4];
#pragma unroll
    for (int kk = 0; kk < 4; ++kk) af[kk] = *reinterpret_cast<const u32x4*>(reinterpret_cast<const char*>(ldsu) + recr + kk * 64);
    asm volatile("" : "+v"(af[0]), "+v"(af[1]), "+v"(af[2]), "+v"(af[3]));
    f32x4 d0 = {0.f, 0.f, 0.f, 0.f}, d1 = {0.f, 0.f, 0.f, 0.f};
#pragma unroll
    for (int kk = 0; kk < 4; ++kk) { d0 = mfma(af[kk], wov[2 * kk], d0); d1 = mfma(af[kk], wov[2 * kk + 1], d1); }
    const float y0 = (d0[0] + d0[1]) + (d0[2] + d0[3]), y1 = (d1[0] + d1[1]) + (d1[2] + d1[3]);
    const float y = x16_sum(sw32_sum(y0, y1)) + fsum;
    const unsigned ya = __float_as_uint(y), yb = __float_as_uint(dpp<XOR1>(y));
    const unsigned w0 = __builtin_amdgcn_perm(yb, ya, 0x03020107u);
    const unsigned w1 = __builtin_amdgcn_perm(yb, tag, 0x06050100u);
    if ((lane & 17) == 0) st8(ws + WS_Y + (size_t)cid * 128, yoff, u32x2{w0, w1});
  }
  if ((b & 1) == 0 && b < 2 * NRED) {
    const int rb = b >> 1;
    const char* ab = ws + WS_Y + (size_t)rb * (NWG * 128);
    const unsigned aoff = (tid >> 6) * 2048 + lane * 16;
    const unsigned t16 = tag & 0xffffu;
    u32x4 va[2];
    for (unsigned spins = 0; spins < SPIN_CAP; ++spins) {
      GLD(va[0], aoff, ab, 0, "sc1"); GLD(va[1], aoff, ab, 1024, "sc1");
      WAITVM(0); PIN(va[0]); PIN(va[1]);
      if (__all((va[0][1] & 0xffffu) == t16 && (va[0][3] & 0xffffu) == t16 && (va[1][1] & 0xffffu) == t16 && (va[1][3] & 0xffffu) == t16)) break;
    }
    float a[4];
#pragma unroll
    for (int k = 0; k < 2; ++k) {
      a[2 * k] = __uint_as_float(va[0][2 * k] & 0xffffff00u) + __uint_as_float(va[1][2 * k] & 0xffffff00u);
      a[2 * k + 1] = __uint_as_float(__builtin_amdgcn_perm(va[0][2 * k], va[0][2 * k + 1], 0x0403020cu))
                   + __uint_as_float(__builtin_amdgcn_perm(va[1][2 * k], va[1][2 * k + 1], 0x0403020cu));
    }
    float s = sw16_sum(sw32_sum(a[0], a[2]), sw32_sum(a[1], a[3]));
    s += dpp<XOR8>(s);
    if (c < 8) lds[L_RA + wave * 32 + c * 4 + r] = s;
    BARRIER();
    if (wave == 0) {
      const int col = lane & 31, hf = lane >> 5;
      const unsigned xv = reinterpret_cast<const unsigned short*>(ldsu + L_X)[rb * 32 + col];
      float sa = 0.f;
#pragma unroll
      for (int w = 0; w < 8; ++w) sa += lds[L_RA + (hf * 8 + w) * 32 + col];
      const float res = bflo(xv) + x32_sum(sa);
      if (lane < 32) *reinterpret_cast<unsigned short*>(out + (rb * 32 + col) * 2) = (unsigned short)(cvtpk(res, res) & 0xffffu);
      if (rb == 0 && lane == 0) __hip_atomic_store(reinterpret_cast<unsigned*>(ws + WS_E), tag, __ATOMIC_RELAXED, __HIP_MEMORY_SCOPE_AGENT);
    }
  }
}

extern "C" void kernel_launch(void* const* d_in, const int* in_sizes, int n_in, void* d_out, int out_size, void* d_ws, size_t ws_size,
                              hipStream_t stream) {
  fused_kernel<<<NWG, NT, 0, stream>>>((const char*)d_in[0], (const char*)d_in[1], (const char*)d_in[2], (const char*)d_in[3], (const char*)d_in[4],
                                       (const char*)d_in[6], (const char*)d_in[7], (const char*)d_in[8], (char*)d_ws, (char*)d_out);
}
```
